```python
import torch

def _softmax(x, axis=-1):
    return torch.softmax(x, dim=axis)
def _gelu(x):
    return torch.nn.functional.gelu(x, approximate="tanh")
def _silu(x):
    return torch.nn.functional.silu(x)
def _sigmoid(x):
    return torch.sigmoid(x)
def _layernorm(x, axis=-1, eps=1e-5):
    m = x.mean(dim=axis, keepdim=True)
    v = x.var(dim=axis, keepdim=True, unbiased=False)
    return (x - m) / torch.sqrt(v + eps)
def _scatter_add(tgt, idx, src, axis=0):
    return tgt.clone().index_add_(axis, idx, src)
def _segment_sum(src, seg, n):
    out = torch.zeros((n,) + src.shape[1:], dtype=src.dtype)
    return out.index_add_(0, seg, src)
def _pad(x, pw):
    flat = tuple(v for pair in reversed(pw) for v in pair)
    return torch.nn.functional.pad(x, flat)

kernel_name = 'gated_mix__mha_block__sliding_local_dilated__prompt'

def setup_inputs():

    _w = (lambda a: (lambda t: t.to(torch.bfloat16) if t.is_floating_point() else t)(torch.as_tensor(a)))
    def _A_setup():
        B, S, D, H = 1, 2048, 448, 16
        return {
            "x": _w((lambda *s: torch.randn(*s, dtype=torch.bfloat16) * 0.05)(B, S, D)),
            "wqkv": _w((lambda *s: torch.randn(*s, dtype=torch.bfloat16) * 1.6)(D, 3 * D)),
            "wo": _w((lambda *s: torch.randn(*s, dtype=torch.bfloat16) * 0.1)(D, D)),
        }
    _A_in = _A_setup()

    def _B_setup():
        B, S, D = 1, 2048, 448
        return {
            "x": _w((lambda *s: torch.randn(*s, dtype=torch.bfloat16) * 0.05)(B, S, D)),
            "wqkv": _w((lambda *s: torch.randn(*s, dtype=torch.bfloat16) * 1.6)(D, 3 * D)),
            "wo": _w((lambda *s: torch.randn(*s, dtype=torch.bfloat16) * 0.05)(D, D)),
        }
    _B_in = _B_setup()

    return {
        "A_x": _A_in["x"],
        "A_wqkv": _A_in["wqkv"],
        "A_wo": _A_in["wo"],
        "B_x": _B_in["x"],
        "B_wqkv": _B_in["wqkv"],
        "B_wo": _B_in["wo"],
        "gate_w": _w((lambda *s: torch.randn(*s, dtype=torch.bfloat16) * 0.05)(448) * 16.0),
    }

def reference(A_x, A_wqkv, A_wo, B_x, B_wqkv, B_wo, gate_w):
    def _A_ref(x, wqkv, wo):
        B, S, D = x.shape
        H = 16
        Dh = D // H
        qkv = (lambda x, *d: x.permute(*d))((x @ wqkv).reshape(B, S, 3, H, Dh), 2, 0, 3, 1, 4)
        q, k, v = qkv[0], qkv[1], qkv[2]
        scores = torch.einsum("bhqd,bhkd->bhqk", q, k) * (Dh ** -0.5)
        p = _softmax(scores, axis=-1)
        o = torch.einsum("bhqk,bhkd->bhqd", p, v)
        return (lambda x, *d: x.permute(*d))(o, 0, 2, 1, 3).reshape(B, S, D) @ wo

    def _B_ref(x, wqkv, wo):
        B, S, D = x.shape
        H = 16; Dh = D // H
        W, dil = 8, 2
        qkv = (lambda x, *d: x.permute(*d))((x @ wqkv).reshape(B, S, 3, H, Dh), 2, 0, 3, 1, 4)
        q, k, v = qkv[0], qkv[1], qkv[2]
        scale = Dh ** -0.5
        pos = torch.arange(S)
        off = torch.arange(W) * dil
        idx = pos[:, None] - off[None, :]
        mask = idx >= 0
        idx_c = torch.where(mask, idx, 0).reshape(-1)
        kg = k[:, :, idx_c, :].reshape(B, H, S, W, Dh)
        vg = v[:, :, idx_c, :].reshape(B, H, S, W, Dh)
        logits = torch.einsum("bhsd,bhswd->bhsw", q, kg) * scale
        logits = torch.where(mask[None, None, :, :], logits, -1e9)
        attn = _softmax(logits, axis=-1)
        o = torch.einsum("bhsw,bhswd->bhsd", attn, vg)
        return (lambda x, *d: x.permute(*d))(o, 0, 2, 1, 3).reshape(B, S, D) @ wo

    x = A_x
    g = _sigmoid((x * gate_w).sum(-1, keepdims=True))
    a = _A_ref(x, A_wqkv, A_wo)
    b = _B_ref(x, B_wqkv, B_wo)
    return g * a + (1 - g) * b
```

```cpp
#include <hip/hip_runtime.h>
#include <hip/hip_bf16.h>
typedef __hip_bfloat16 bf16;
constexpr int S = 2048, D = 448, H = 16, DH = 28, N3 = 1344;
__device__ inline float b2f(bf16 v) { return __bfloat162float(v); }
__device__ inline float rb(float v) { return __bfloat162float(__float2bfloat16(v)); }

__global__ void gemm_qkv(const bf16* x, const bf16* w, float* out) {
    int n = blockIdx.x * 64 + threadIdx.x; int r = blockIdx.y;
    float acc = 0;
    for (int k = 0; k < D; ++k) acc += b2f(x[r * D + k]) * b2f(w[k * N3 + n]);
    out[r * N3 + n] = rb(acc);
}
__global__ void attnA(const float* qkv, float* o) {
    int r = blockIdx.x, h = blockIdx.y, t = threadIdx.x;
    __shared__ float q[DH]; __shared__ float red[256]; __shared__ float p[S];
    if (t < DH) q[t] = qkv[r * N3 + h * DH + t];
    __syncthreads();
    float scale = 0.18898223650461362f;
    float mx = -1e30f;
    for (int j = t; j < S; j += 256) { float s = 0; for (int d = 0; d < DH; ++d) s += q[d] * qkv[j * N3 + D + h * DH + d]; s *= scale; p[j] = s; mx = fmaxf(mx, s); }
    red[t] = mx; __syncthreads();
    for (int o2 = 128; o2 > 0; o2 >>= 1) { if (t < o2) red[t] = fmaxf(red[t], red[t + o2]); __syncthreads(); }
    mx = red[0]; __syncthreads();
    float sum = 0;
    for (int j = t; j < S; j += 256) { float e = __expf(p[j] - mx); p[j] = e; sum += e; }
    red[t] = sum; __syncthreads();
    for (int o2 = 128; o2 > 0; o2 >>= 1) { if (t < o2) red[t] += red[t + o2]; __syncthreads(); }
    sum = red[0]; __syncthreads();
    if (t < DH) { float a = 0; for (int j = 0; j < S; ++j) a += p[j] * qkv[j * N3 + 2 * D + h * DH + t]; o[r * D + h * DH + t] = rb(a / sum); }
}
__global__ void attnB(const float* qkv, float* o) {
    int idx = blockIdx.x * 256 + threadIdx.x; int r = idx / H, h = idx % H;
    float scale = 0.18898223650461362f;
    float lg[8]; float mx = -1e30f;
    for (int w = 0; w < 8; ++w) { int j = r - 2 * w; float s = -1e9f; if (j >= 0) { s = 0; for (int d = 0; d < DH; ++d) s += qkv[r * N3 + h * DH + d] * qkv[j * N3 + D + h * DH + d]; s *= scale; } lg[w] = s; mx = fmaxf(mx, s); }
    float sum = 0; for (int w = 0; w < 8; ++w) { lg[w] = __expf(lg[w] - mx); sum += lg[w]; }
    for (int d = 0; d < DH; ++d) { float a = 0; for (int w = 0; w < 8; ++w) { int j = r - 2 * w; if (j < 0) j = 0; a += lg[w] * qkv[j * N3 + 2 * D + h * DH + d]; } o[r * D + h * DH + d] = rb(a / sum); }
}
__global__ void outp(const bf16* x, const bf16* gw, const float* oA, const float* oB, const bf16* woA, const bf16* woB, bf16* out) {
    int n = blockIdx.x * 64 + threadIdx.x; int r = blockIdx.y;
    float lg = 0; for (int k = 0; k < D; ++k) lg += b2f(x[r * D + k]) * b2f(gw[k]);
    float g = 1.f / (1.f + __expf(-lg));
    float a = 0, b = 0;
    for (int k = 0; k < D; ++k) { a += oA[r * D + k] * b2f(woA[k * D + n]); b += oB[r * D + k] * b2f(woB[k * D + n]); }
    out[r * D + n] = __float2bfloat16(g * a + (1.f - g) * b);
}
extern "C" void kernel_launch(void* const* d_in, const int* in_sizes, int n_in, void* d_out, int out_size, void* d_ws, size_t ws_size, hipStream_t stream) {
    const bf16* x = (const bf16*)d_in[0]; const bf16* wA = (const bf16*)d_in[1]; const bf16* woA = (const bf16*)d_in[2];
    const bf16* wB = (const bf16*)d_in[4]; const bf16* woB = (const bf16*)d_in[5]; const bf16* gw = (const bf16*)d_in[6];
    float* qA = (float*)d_ws; float* qB = qA + S * N3; float* oA = qB + S * N3; float* oB = oA + S * D;
    gemm_qkv<<<dim3(N3 / 64, S), 64, 0, stream>>>(x, wA, qA);
    gemm_qkv<<<dim3(N3 / 64, S), 64, 0, stream>>>(x, wB, qB);
    attnA<<<dim3(S, H), 256, 0, stream>>>(qA, oA);
    attnB<<<S * H / 256, 256, 0, stream>>>(qB, oB);
    outp<<<dim3(D / 64, S), 64, 0, stream>>>(x, gw, oA, oB, woA, woB, (bf16*)d_out);
}
```

```cpp
#include <hip/hip_runtime.h>
#include <hip/hip_bf16.h>
#include <stdint.h>

typedef __bf16 bf16x8_t __attribute__((ext_vector_type(8)));
typedef __bf16 bf16x2_t __attribute__((ext_vector_type(2)));
typedef float f32x2 __attribute__((ext_vector_type(2)));
typedef float f32x4 __attribute__((ext_vector_type(4)));
typedef float f32x16 __attribute__((ext_vector_type(16)));
typedef unsigned u32x4 __attribute__((ext_vector_type(4)));
typedef unsigned u32x2 __attribute__((ext_vector_type(2)));
typedef short s16x4 __attribute__((ext_vector_type(4)));
typedef unsigned long long u64;
#define LAS __attribute__((address_space(3)))
#define GAS __attribute__((address_space(1)))
#define DI __device__ __forceinline__
#define SBAR() __builtin_amdgcn_sched_barrier(0)
#define RLX_AGENT __ATOMIC_RELAXED, __HIP_MEMORY_SCOPE_AGENT

constexpr int SEQ = 2048, DM = 448, N3 = 1344;
constexpr int XROW = DM * 2;
constexpr int WROW = N3 * 2;
constexpr float QSCALE = 0.18898223650461362f * 1.4426950408889634f;

constexpr unsigned WS_EPOCH = 0;
constexpr unsigned WS_FKV = 4096;
constexpr unsigned WS_FOB = WS_FKV + 16384;
constexpr unsigned WS_KV = 65536;
constexpr unsigned WS_OA = WS_KV + 4194304;
constexpr unsigned WS_OB = WS_OA + 2097152;
constexpr unsigned WS_END = WS_OB + 2097152;
#define STAMP(i) do {} while (0)

constexpr unsigned RB_ = 0, RC_ = 28672, RD_ = 57344, RA_ = 86016, RE_ = 114688;
constexpr unsigned HALO = 143360, HROW = 912;
constexpr unsigned GATE = HALO + 16 * HROW;
constexpr unsigned GBUF = GATE + 4 * 896;
constexpr unsigned MBUF = GBUF + 512;
constexpr unsigned LDS_BYTES = 163840;
constexpr unsigned KVS0 = RE_, KVS1 = RE_ + 16384, QX = RE_ + 16384, XB = RA_, JUNK = RE_ + 32768;

DI unsigned cvtpk(float a, float b) { f32x2 v = {a, b}; return __builtin_bit_cast(unsigned, __builtin_convertvector(v, bf16x2_t)); }
DI f32x16 mfma(u32x4 a, u32x4 b, f32x16 c) { return __builtin_amdgcn_mfma_f32_32x32x16_bf16(__builtin_bit_cast(bf16x8_t, a), __builtin_bit_cast(bf16x8_t, b), c, 0, 0, 0); }
DI u32x4 pack8(const f32x16& a, int s) { return (u32x4){cvtpk(a[8 * s], a[8 * s + 1]), cvtpk(a[8 * s + 2], a[8 * s + 3]), cvtpk(a[8 * s + 4], a[8 * s + 5]), cvtpk(a[8 * s + 6], a[8 * s + 7])}; }
DI u32x4 ldsr(const LAS char* p) { return *(const LAS u32x4*)p; }
DI void ldsw(LAS char* p, u32x4 v) { *(LAS u32x4*)p = v; }
DI u32x2 trr(const LAS char* p) { return __builtin_bit_cast(u32x2, __builtin_amdgcn_ds_read_tr16_b64_v4i16((LAS s16x4*)p)); }
DI u32x4 tr2(const LAS char* p0, const LAS char* p1) { u32x2 a = trr(p0), b = trr(p1); return (u32x4){a[0], a[1], b[0], b[1]}; }
DI float ex2(float x) { return __builtin_amdgcn_exp2f(x); }
DI float swap_max(float m) { auto rr = __builtin_amdgcn_permlane32_swap(__float_as_uint(m), __float_as_uint(m), false, false); return fmaxf(__uint_as_float(rr[0]), __uint_as_float(rr[1])); }
DI float from_hi(float v) { auto rr = __builtin_amdgcn_permlane32_swap(__float_as_uint(v), __float_as_uint(v), false, false); return __uint_as_float(rr[1]); }
DI u32x4 mksrd(const void* p, unsigned bytes) { u64 a = (u64)p; return (u32x4){(unsigned)a, (unsigned)(a >> 32) & 0xffffu, bytes, 0x00020000u}; }
DI void dma16(u32x4 srd, unsigned voff, unsigned soff, unsigned ldsaddr) {
    unsigned keep;
    asm volatile("s_mov_b32 %0, m0\n\ts_mov_b32 m0, %4\n\ts_nop 0\n\tbuffer_load_dwordx4 %1, %2, %3 offen lds\n\ts_mov_b32 m0, %0"
                 : "=&s"(keep) : "v"(voff), "s"(srd), "s"(soff), "s"(ldsaddr) : "memory");
}
template <int N> DI void wait_vm() { asm volatile("s_waitcnt vmcnt(%0)" ::"n"(N) : "memory"); }
DI void bar() { asm volatile("s_waitcnt lgkmcnt(0)\n\ts_barrier" ::: "memory"); }
#define MX3(a, b, c) __builtin_fmaxf(__builtin_fmaxf((a), (b)), (c))
DI float max16(const f32x16& p) {
    float a = MX3(p[0], p[1], p[2]), b = MX3(p[3], p[4], p[5]);
    a = MX3(a, p[6], p[7]); b = MX3(b, p[8], p[9]); a = MX3(a, p[10], p[11]); b = MX3(b, p[12], p[13]); a = MX3(a, p[14], p[15]);
    return __builtin_fmaxf(a, b);
}
DI void packK(const f32x16& a, int hh, u32x4& f0, u32x4& f1) { f0 = pack8(a, 0); f1 = pack8(a, 1); if (hh) { f1[2] = 0x00003F80u; f1[3] = 0u; } }
DI void packV(const f32x16& a, int r, u32x4& f0, u32x4& f1) {
    f0 = pack8(a, 0); f1 = pack8(a, 1);
    if (r >= 28) { unsigned c = r == 28 ? 0x3F803F80u : 0u; f0 = (u32x4){c, c, c, c}; f1 = f0; }
}
constexpr unsigned kSpin = 1u << 18;

__global__ __launch_bounds__(512, 2) void fused_kernel(const char* __restrict__ x, const char* __restrict__ wA, const char* __restrict__ woA,
                                                       const char* __restrict__ wB, const char* __restrict__ woB, const char* __restrict__ gw,
                                                       char* __restrict__ out, char* ws) {
    __shared__ __attribute__((aligned(16))) char lds_[LDS_BYTES];
    LAS char* lds = (LAS char*)lds_;
    const unsigned L0 = (unsigned)(size_t)lds;
    STAMP(0);
    const int lane = threadIdx.x & 63, r = lane & 31, hh = lane >> 5;
    const int wave = __builtin_amdgcn_readfirstlane(threadIdx.x >> 6), w4 = wave & 3;
    const bool roleQ = wave >= 4;
    const int xcd = blockIdx.x & 7, idx = blockIdx.x >> 3;
    const int h = (xcd & 3) * 4 + (idx >> 3), rb = (xcd >> 2) * 8 + (idx & 7), r0 = rb * 128;

    const u32x4 sX = mksrd(x, SEQ * XROW), sWA = mksrd(wA, DM * WROW), sWB = mksrd(wB, DM * WROW);
    const __amdgpu_buffer_rsrc_t rWS = __builtin_amdgcn_make_buffer_rsrc(ws, 0, WS_END, 0x00020000);

    const unsigned vX0 = (r0 + 16 * wave + (lane >> 3)) * XROW + (((lane & 7) ^ (lane >> 4)) << 4);
    const unsigned vX1 = (r0 + 16 * wave + 8 + (lane >> 3)) * XROW + (((lane & 7) ^ (lane >> 4) ^ 4) << 4);
    const unsigned vW = (16 * w4 + (lane >> 2)) * WROW + h * 56 + (lane & 3) * 16;
#define LD_X(c, slot) do { dma16(sX, vX0, (c) * 128, L0 + (slot) + wave * 2048); dma16(sX, vX1, (c) * 128, L0 + (slot) + wave * 2048 + 1024); } while (0)
#define LD_W(srd, c, u, dst) dma16(srd, vW, (c) * 64 * WROW + (u) * XROW, L0 + (dst) + (u) * 4096 + w4 * 1024)
#define LD_CH_A(c, slot) do { LD_X(c, slot); LD_W(sWA, c, 1, (slot) + 16384); LD_W(sWA, c, 2, (slot) + 16384); } while (0)
#define LD_CH_Q(c, slot) do { LD_X(c, slot); LD_W(sWA, c, 0, (slot) + 16384); } while (0)
#define LD_WB(c, dst) do { LD_W(sWB, c, 0, dst); LD_W(sWB, c, 1, dst); LD_W(sWB, c, 2, dst); } while (0)

    const int xrow = 32 * w4 + r, xf = (xrow >> 1) & 7;
    const LAS char* xa[4];
#pragma unroll
    for (int s = 0; s < 4; ++s) xa[s] = lds + xrow * 128 + (((2 * s + hh) ^ xf) << 4);
    const LAS char* wa = lds + (8 * hh + ((lane & 15) >> 2)) * 64 + ((lane >> 4) & 1) * 32 + (lane & 3) * 8;
#define OPQ(p) asm volatile("" : "+v"(p))
#define WFR(wb, u, s) tr2((wb) + (u) * 4096 + (s) * 1024, (wb) + (u) * 4096 + (s) * 1024 + 256)
#define XFR(c, s, slot) do { const LAS char* xp_ = xa[s] + ((slot) & ~0xffffu); if ((slot) >> 16) OPQ(xp_); xr[4 * (c) + (s)] = ldsr(xp_ + ((slot) & 0xffffu)); } while (0)

    u32x4 xr[28];
    u64 tag;
    u32x4 q0, q1;
    u32x4 b0, b1;
    float gq;
    u32x4 sg[3][3];
    const unsigned vKV = WS_KV + h * 262144 + wave * 2048 + lane * 16;
    const unsigned vO = (lane >> 2) * XROW + (h & 7) * 112 + (lane & 3) * 16;
#define LD_KV(i, g) do { const unsigned so_ = (g) < 16 ? (unsigned)(((rb + (g)) & 15) << 14) : WS_END; \
        sg[i][0] = __builtin_amdgcn_raw_buffer_load_b128(rWS, vKV, so_, 16); sg[i][1] = __builtin_amdgcn_raw_buffer_load_b128(rWS, vKV + 1024, so_, 16); \
        const int P_ = ((g) - 1) * 8 + wave, im_ = P_ / 28, pc_ = P_ - im_ * 28; \
        sg[i][2] = __builtin_amdgcn_raw_buffer_load_b128(__builtin_amdgcn_make_buffer_rsrc((void*)(im_ < 2 ? woB : woA), 0, (g) < 15 ? DM * XROW : 0, 0x00020000), vO, pc_ * 16 * XROW + (im_ & 1) * 64, 0); } while (0)
    const GAS u64* fkv_poll = (const GAS u64*)(ws + WS_FKV + h * 1024 + (lane >> 2) * 64 + (lane & 3) * 8);
    f32x16 O = {};
    f32x16 Sp;
    u32x4 vp0, vp1;
    float mref;
    const LAS char* kvl = lds + lane * 16;

    if (!roleQ) {
        LD_CH_A(0, RD_); LD_CH_A(1, RE_); LD_CH_A(2, RA_); LD_CH_A(3, RB_); LD_CH_A(4, RC_);
        {
            const u32x4 ones = {~0u, ~0u, ~0u, ~0u}; const unsigned vo = WS_OA + ((rb * 4 + w4) * 16 + h) * 2048 + lane * 16;
            __builtin_amdgcn_raw_buffer_store_b128(ones, rWS, vo, 0, 16); __builtin_amdgcn_raw_buffer_store_b128(ones, rWS, vo + 1024, 0, 16);
        }
        { u64 e; asm volatile("s_load_dwordx2 %0, %1, 0x0\n\ts_waitcnt lgkmcnt(0)" : "=s"(e) : "s"(ws) : "memory"); tag = e * 0x9E3779B97F4A7C15ull + 0x632BE59BD9B4E019ull; }
        f32x16 aK = {}, aV = {};
#define CMP_A(c, slot) do { const LAS char* wb_ = wa + (slot) + 16384; OPQ(wb_); _Pragma("unroll") for (int s = 0; s < 4; ++s) { XFR(c, s, slot); \
            aK = mfma(WFR(wb_, 1, s), xr[4 * (c) + s], aK); aV = mfma(xr[4 * (c) + s], WFR(wb_, 2, s), aV); } } while (0)
        wait_vm<18>(); bar(); STAMP(1); CMP_A(0, RD_);
        wait_vm<14>(); bar(); LD_CH_A(5, RD_); CMP_A(1, RE_);
        wait_vm<14>(); bar(); LD_CH_A(6, RE_); CMP_A(2, RA_);
        wait_vm<14>(); bar(); CMP_A(3, RB_);
        wait_vm<10>(); bar(); STAMP(2); CMP_A(4, RC_);
        wait_vm<4>(); bar(); CMP_A(5, RD_);
        wait_vm<0>(); bar(); CMP_A(6, RE_);
        u32x4 k0, k1, v0, v1;
        packK(aK, hh, k0, k1); packV(aV, r, v0, v1);
        { const unsigned vo = WS_KV + (h * 16 + rb) * 16384 + w4 * 4096 + lane * 16;
          __builtin_amdgcn_raw_buffer_store_b128(k0, rWS, vo, 0, 16); __builtin_amdgcn_raw_buffer_store_b128(k1, rWS, vo + 1024, 0, 16);
          __builtin_amdgcn_raw_buffer_store_b128(v0, rWS, vo + 2048, 0, 16); __builtin_amdgcn_raw_buffer_store_b128(v1, rWS, vo + 3072, 0, 16); }
        STAMP(3);
        bar();
        { LAS char* d = lds + KVS0 + w4 * 4096 + lane * 16; ldsw(d, k0); ldsw(d + 1024, k1); ldsw(d + 2048, v0); ldsw(d + 3072, v1); }
        aK = (f32x16){}; aV = (f32x16){};
#define CMP_B(c, img) do { const LAS char* wb_ = wa + (img); OPQ(wb_); _Pragma("unroll") for (int s = 0; s < 4; ++s) { \
            aK = mfma(WFR(wb_, 1, s), xr[4 * (c) + s], aK); aV = mfma(xr[4 * (c) + s], WFR(wb_, 2, s), aV); } } while (0)
        CMP_B(0, RA_); CMP_B(1, RA_ + 12288);
        wait_vm<0>();
        if (lane == 0) __hip_atomic_store((GAS u64*)(ws + WS_FKV + (h * 16 + rb) * 64 + w4 * 8), tag, RLX_AGENT);
        STAMP(4);
        bar();
        q0 = ldsr(lds + QX + w4 * 2048 + lane * 16); q1 = ldsr(lds + QX + w4 * 2048 + 1024 + lane * 16);
        gq = *(const LAS float*)(lds + GBUF + (32 * w4 + r) * 4);
        CMP_B(2, RB_); CMP_B(3, RB_ + 12288);
        bar();
        CMP_B(4, RC_); CMP_B(5, RC_ + 12288);
        bar();
        CMP_B(6, RD_);
        packK(aK, hh, k0, k1); packV(aV, r, v0, v1);
        { LAS char* d = lds + XB + (w4 + 1) * 4096 + lane * 16; ldsw(d, k0); ldsw(d + 1024, k1); ldsw(d + 2048, v0); ldsw(d + 3072, v1); }
    } else {
        if (lane < 56) dma16(mksrd(gw, XROW), lane * 16, 0, L0 + GATE + w4 * XROW);
        LD_CH_Q(0, RD_); LD_CH_Q(1, RE_); LD_CH_Q(2, RA_); LD_CH_Q(3, RB_); LD_CH_Q(4, RC_);
        if (lane < 56) {
#pragma unroll
            for (int i = 0; i < 4; ++i) { const int hr = 4 * w4 + i, gr = r0 - 16 + hr; dma16(sX, lane * 16, (gr < 0 ? 0 : gr) * XROW, L0 + HALO + hr * HROW); }
        }
        { u64 e; asm volatile("s_load_dwordx2 %0, %1, 0x0\n\ts_waitcnt lgkmcnt(0)" : "=s"(e) : "s"(ws) : "memory"); tag = e * 0x9E3779B97F4A7C15ull + 0x632BE59BD9B4E019ull; }
        f32x16 aQ = {};
#define PATCH(c, slot) do { if (lane < 16) { const unsigned short gv = *(const LAS unsigned short*)(lds + GATE + w4 * XROW + ((c) * 64 + 16 * w4 + lane) * 2); \
            *(LAS unsigned short*)(lds + (slot) + 16384 + (16 * w4 + lane) * 64 + 56) = gv; } } while (0)
#define CMP_Q(c, slot) do { const LAS char* wb_ = wa + (slot) + 16384; OPQ(wb_); _Pragma("unroll") for (int s = 0; s < 4; ++s) { XFR(c, s, slot); aQ = mfma(WFR(wb_, 0, s), xr[4 * (c) + s], aQ); } } while (0)
        wait_vm<16>(); PATCH(0, RD_); bar(); STAMP(1); CMP_Q(0, RD_);
        wait_vm<13>(); PATCH(1, RE_); bar(); LD_CH_Q(5, RD_); CMP_Q(1, RE_);
        wait_vm<13>(); PATCH(2, RA_); bar(); LD_CH_Q(6, RE_); CMP_Q(2, RA_);
        wait_vm<13>(); PATCH(3, RB_); bar(); LD_WB(0, RA_); LD_WB(1, RA_ + 12288); CMP_Q(3, RB_);
        wait_vm<16>(); PATCH(4, RC_); bar(); STAMP(2); LD_WB(2, RB_); LD_WB(3, RB_ + 12288); CMP_Q(4, RC_);
        wait_vm<15>(); PATCH(5, RD_); bar(); LD_WB(4, RC_); LD_WB(5, RC_ + 12288); CMP_Q(5, RD_);
        wait_vm<18>(); PATCH(6, RE_); bar(); LD_WB(6, RD_); CMP_Q(6, RE_);
        gq = 1.f / (1.f + __expf(-from_hi(aQ[12])));
#pragma unroll
        for (int i = 0; i < 16; ++i) aQ[i] *= QSCALE;
        q0 = pack8(aQ, 0); q1 = pack8(aQ, 1);
        if (hh) { q1[2] = 0u; q1[3] = 0u; }
        STAMP(3);
        wait_vm<15>(); bar();
        ldsw(lds + QX + w4 * 2048 + lane * 16, q0); ldsw(lds + QX + w4 * 2048 + 1024 + lane * 16, q1);
        if (!hh) *(LAS float*)(lds + GBUF + (32 * w4 + r) * 4) = gq;
        aQ = (f32x16){};
        f32x4 aH = {};
        const LAS char* ha = lds + HALO + (lane & 15) * HROW + (lane >> 4) * 16; OPQ(ha);
        const unsigned hwo = (1 + (w4 >> 1)) * 4096 + (w4 & 1) * 32 + (8 * (lane >> 4) + ((lane & 15) >> 2)) * 64 + (lane & 3) * 8;
#define MFMA16(a, b, c) __builtin_amdgcn_mfma_f32_16x16x32_bf16(__builtin_bit_cast(bf16x8_t, a), __builtin_bit_cast(bf16x8_t, b), c, 0, 0, 0)
#define CMP_QB(c, img) do { const LAS char* wb_ = wa + (img); OPQ(wb_); const LAS char* hb_ = lds + hwo + (img); OPQ(hb_); \
            _Pragma("unroll") for (int s = 0; s < 4; ++s) { aQ = mfma(WFR(wb_, 0, s), xr[4 * (c) + s], aQ); \
                if (s & 1) aH = MFMA16(tr2(hb_ + (s >> 1) * 2048, hb_ + (s >> 1) * 2048 + 256), ldsr(ha + (2 * (c) + (s >> 1)) * 64), aH); } } while (0)
        CMP_QB(0, RA_); CMP_QB(1, RA_ + 12288);
        STAMP(4);
        wait_vm<9>(); bar();
        CMP_QB(2, RB_); CMP_QB(3, RB_ + 12288);
        wait_vm<3>(); bar();
        CMP_QB(4, RC_); CMP_QB(5, RC_ + 12288);
        wait_vm<0>(); bar();
        CMP_QB(6, RD_);
        {   const int k16 = lane & 15, g4 = lane >> 4; const bool pad = (w4 & 1) && g4 == 3;
            u32x2 pk = {cvtpk(aH[0], aH[1]), cvtpk(aH[2], aH[3])};
            if (w4 < 2) { if (pad) pk = (u32x2){0u, 0u};
                *(LAS u32x2*)(lds + XB + (w4 & 1) * 1024 + (16 + k16 + 32 * (g4 & 1)) * 16 + (g4 >> 1) * 8) = pk;
            } else { if (pad) pk = (u32x2){0x00003F80u, 0u};
                LAS unsigned short* d = (LAS unsigned short*)(lds + XB + 3072 + (16 * (w4 & 1) + 4 * g4 + 32 * ((k16 >> 2) & 1)) * 16 + (4 * (k16 >> 3) + (k16 & 3)) * 2);
                d[0] = (unsigned short)pk[0]; d[8] = (unsigned short)(pk[0] >> 16); d[16] = (unsigned short)pk[1]; d[24] = (unsigned short)(pk[1] >> 16); }
        }
#pragma unroll
        for (int i = 0; i < 16; ++i) aQ[i] *= QSCALE;
        b0 = pack8(aQ, 0); b1 = pack8(aQ, 1);
        if (hh) { b1[2] = 0u; b1[3] = 0u; }
    }
    u64 fl = __hip_atomic_load(fkv_poll, RLX_AGENT);
    STAMP(5);
    bar();
    for (unsigned sp = 0; sp < kSpin; ++sp) { if (__all(fl == tag)) break; __builtin_amdgcn_s_sleep(1); fl = __hip_atomic_load(fkv_poll, RLX_AGENT); }
    LD_KV(0, 1); LD_KV(1, 2); LD_KV(2, 3);
    STAMP(6);
    u32x4 pp1;
    u32x4 vq1;
    if (!roleQ) {
        const LAS char* t0 = kvl + KVS0; OPQ(t0);
        f32x16 S = mfma(ldsr(t0), q0, (f32x16){}); S = mfma(ldsr(t0 + 1024), q1, S);
        const float m = swap_max(max16(S));
        const unsigned mb = cvtpk(m, 0.f) & 0xffffu; mref = __uint_as_float(mb << 16);
        if (hh) q1[2] = mb ^ 0x8000u;
        Sp = mfma(ldsr(t0 + 4096), q0, (f32x16){}); Sp = mfma(ldsr(t0 + 5120), q1, Sp);
#pragma unroll
        for (int i = 0; i < 16; ++i) S[i] = ex2(S[i] - mref);
        O = mfma(ldsr(t0 + 2048), pack8(S, 0), O); O = mfma(ldsr(t0 + 3072), pack8(S, 1), O);
        S = mfma(ldsr(t0 + 8192), q0, (f32x16){}); S = mfma(ldsr(t0 + 9216), q1, S);
#pragma unroll
        for (int i = 0; i < 16; ++i) Sp[i] = ex2(Sp[i]);
        O = mfma(ldsr(t0 + 6144), pack8(Sp, 0), O); pp1 = pack8(Sp, 1); vq1 = ldsr(t0 + 7168);
        Sp = S; vp0 = ldsr(t0 + 10240); vp1 = ldsr(t0 + 11264);
    } else {
        {   const LAS char* tp = kvl + XB + w4 * 4096; OPQ(tp); const LAS char* to = tp + 4096;
            f32x16 A = mfma(ldsr(tp), b0, (f32x16){}); A = mfma(ldsr(tp + 1024), b1, A);
            f32x16 B = mfma(ldsr(to), b0, (f32x16){}); B = mfma(ldsr(to + 1024), b1, B);
            const u64 M = 0x5555ull << (18 + r);
            unsigned Mp = (unsigned)M >> (4 * hh); const unsigned Mo = (unsigned)(M >> 32) >> (4 * hh);
            if (rb == 0 && w4 == 0) Mp = 0u;
            const float NEG = -__builtin_inff();
#pragma unroll
            for (int i = 0; i < 16; ++i) { const int c = (i & 3) + 8 * (i >> 2); B[i] = ((Mo >> c) & 1u) ? B[i] : NEG; A[i] = (i >= 8 && ((Mp >> c) & 1u)) ? A[i] : NEG; }
            float m = max16(B);
            m = MX3(m, A[8], A[9]); m = MX3(m, A[10], A[11]); m = MX3(m, A[12], A[13]); m = MX3(m, A[14], A[15]);
            m = swap_max(m);
#pragma unroll
            for (int i = 0; i < 16; ++i) { B[i] = ex2(B[i] - m); if (i >= 8) A[i] = ex2(A[i] - m); }
            f32x16 OB = mfma(ldsr(tp + 3072), pack8(A, 1), (f32x16){});
            OB = mfma(ldsr(to + 2048), pack8(B, 0), OB); OB = mfma(ldsr(to + 3072), pack8(B, 1), OB);
            const float sc = (1.f - gq) / from_hi(OB[12]);
#pragma unroll
            for (int i = 0; i < 16; ++i) OB[i] *= sc;
            u32x4 f0 = pack8(OB, 0), f1 = pack8(OB, 1);
            if (hh) { f1[2] = 0u; f1[3] = 0u; }
            const unsigned vo = WS_OB + ((rb * 4 + w4) * 16 + h) * 2048 + lane * 16;
            __builtin_amdgcn_raw_buffer_store_b128(f0, rWS, vo, 0, 16); __builtin_amdgcn_raw_buffer_store_b128(f1, rWS, vo + 1024, 0, 16);
        }
        const LAS char* t0 = kvl + KVS0 + 12288; OPQ(t0);
        Sp = mfma(ldsr(t0), q0, (f32x16){}); Sp = mfma(ldsr(t0 + 1024), q1, Sp);
        const float m = swap_max(max16(Sp));
        const unsigned mb = cvtpk(m, 0.f) & 0xffffu; mref = __uint_as_float(mb << 16);
        if (hh) q1[2] = mb ^ 0x8000u;
#pragma unroll
        for (int i = 0; i < 16; ++i) Sp[i] -= mref;
        vp0 = ldsr(t0 + 2048); vp1 = ldsr(t0 + 3072);
        pp1 = (u32x4){0u, 0u, 0u, 0u}; vq1 = pp1;
    }
    STAMP(7);
    {
        LAS char* stw = lds + KVS0 + wave * 2048 + lane * 16; OPQ(stw);
        LAS char* wow = lds + wave * 1024 + lane * 16;
        ldsw(stw + 16384, sg[0][0]); ldsw(stw + 16384 + 1024, sg[0][1]); ldsw(wow, sg[0][2]);
        LD_KV(0, 4);
        const LAS char* kt = kvl + KVS0 + (roleQ ? 8192 : 0); OPQ(kt);
#define EX4(Sx, i) do { Sx[i] = ex2(Sx[i]); Sx[i + 1] = ex2(Sx[i + 1]); Sx[i + 2] = ex2(Sx[i + 2]); Sx[i + 3] = ex2(Sx[i + 3]); } while (0)
#define STEP(t, i) do { \
            bar(); \
            const unsigned cur_ = ((t) & 1) << 14; \
            const LAS char* b_ = kt + cur_; \
            const u32x4 ka0 = ldsr(b_), ka1 = ldsr(b_ + 1024), va0 = ldsr(b_ + 2048), va1 = ldsr(b_ + 3072), kb0 = ldsr(b_ + 4096), kb1 = ldsr(b_ + 5120); \
            SBAR(); EX4(Sp, 0); SBAR(); O = mfma(vq1, pp1, O); SBAR(); \
            { LAS char* d_ = stw + ((t) < 15 ? (cur_ ^ 16384u) : junk_kv); ldsw(d_, sg[i][0]); ldsw(d_ + 1024, sg[i][1]); ldsw(wow + ((t) < 14 ? (unsigned)(t) * 8192u : junk_wo), sg[i][2]); } \
            LD_KV(i, (t) + 4); \
            f32x16 Sa; u32x4 p0_; \
            SBAR(); EX4(Sp, 4); p0_ = pack8(Sp, 0); SBAR(); Sa = mfma(ka0, q0, (f32x16){}); SBAR(); \
            EX4(Sp, 8); SBAR(); Sa = mfma(ka1, q1, Sa); SBAR(); \
            EX4(Sp, 12); SBAR(); O = mfma(vp0, p0_, O); SBAR(); \
            pp1 = pack8(Sp, 1); SBAR(); Sp = mfma(kb0, q0, (f32x16){}); vp0 = ldsr(b_ + 6144); SBAR(); \
            EX4(Sa, 0); SBAR(); Sp = mfma(kb1, q1, Sp); SBAR(); \
            EX4(Sa, 4); SBAR(); O = mfma(vp1, pp1, O); vp1 = ldsr(b_ + 7168); SBAR(); \
            p0_ = pack8(Sa, 0); EX4(Sa, 8); SBAR(); O = mfma(va0, p0_, O); SBAR(); \
            EX4(Sa, 12); pp1 = pack8(Sa, 1); vq1 = va1; SBAR(); } while (0)
        const unsigned junk_kv = JUNK - KVS0 - wave * 2048, junk_wo = JUNK - wave * 1024;
        for (int t = 1; t < 16; t += 3) {
            if (t == 13 && roleQ) { wait_vm<0>(); if (lane == 0) __hip_atomic_store((GAS u64*)(ws + WS_FOB + (rb * 16 + h) * 64 + w4 * 8), tag, RLX_AGENT); }
            STEP(t, 1); STEP(t + 1, 2); STEP(t + 2, 0);
        }
        O = mfma(vq1, pp1, O);
#pragma unroll
        for (int i = 0; i < 16; ++i) Sp[i] = ex2(Sp[i]);
        O = mfma(vp0, pack8(Sp, 0), O); O = mfma(vp1, pack8(Sp, 1), O);
    }
    {
        float z = 0.f;
#pragma unroll
        for (int i = 0; i < 16; ++i) z = __builtin_fmaf(O[i], 0.f, z);
        const bool bad = !(z == 0.f) || (hh && !(O[12] <= 1.0e18f));
        if (__any(bad)) {
            if (hh) q1[2] = 0u;
            O = (f32x16){}; mref = -__builtin_inff();
            for (int tt = 0; tt < 64; ++tt) {
                const int g = tt >> 2, ti = tt & 3;
                const bool mine = g == 0 ? (roleQ ? ti == 3 : ti < 3) : ((ti >> 1) == (roleQ ? 1 : 0));
                if (!mine) continue;
                const unsigned vo = WS_KV + h * 262144 + lane * 16, so = (unsigned)(((rb + g) & 15) << 14) + ti * 4096;
                const u32x4 k0 = __builtin_amdgcn_raw_buffer_load_b128(rWS, vo, so, 16), k1 = __builtin_amdgcn_raw_buffer_load_b128(rWS, vo + 1024, so, 16);
                const u32x4 v0 = __builtin_amdgcn_raw_buffer_load_b128(rWS, vo + 2048, so, 16), v1 = __builtin_amdgcn_raw_buffer_load_b128(rWS, vo + 3072, so, 16);
                f32x16 S = mfma(k0, q0, (f32x16){}); S = mfma(k1, q1, S);
                const float mn = fmaxf(mref, swap_max(max16(S))), al = ex2(mref - mn);
                mref = mn;
#pragma unroll
                for (int i = 0; i < 16; ++i) { S[i] = ex2(S[i] - mn); O[i] *= al; }
                O = mfma(v0, pack8(S, 0), O); O = mfma(v1, pack8(S, 1), O);
            }
        }
    }
    STAMP(8);
    const int T = rb * 4 + (h >> 3) * 2 + (w4 & 1), ct = w4 >> 1;
    const LAS char* wo0 = lds + ct * 28672 + (4 * hh + ((lane & 15) >> 2)) * 64 + ((lane >> 4) & 1) * 32 + (lane & 3) * 8;
    wo0 += roleQ ? 0 : 57344;
    const LAS char* wo1 = wo0 + 1536 - hh * 512;
#define WOF(base, hd, s) ((s) == 0 ? tr2(wo0 + (base) + (hd) * 1792, wo0 + (base) + (hd) * 1792 + 512) : tr2(wo0 + (base) + (hd) * 1792 + 1024, wo1 + (base) + (hd) * 1792))
    LAS char* mg = lds + KVS0 + w4 * 4096 + lane * 16; OPQ(mg);
    if (roleQ) {
#pragma unroll
        for (int i = 0; i < 4; ++i) *(LAS f32x4*)(mg + i * 1024) = (f32x4){O[4 * i], O[4 * i + 1], O[4 * i + 2], O[4 * i + 3]};
        if (!hh) *(LAS float*)(lds + MBUF + (32 * w4 + r) * 4) = mref;
        const GAS u64* fp = (const GAS u64*)(ws + WS_FOB + (rb * 16 + (lane & 15)) * 64 + (T & 3) * 8);
        for (unsigned sp = 0; sp < kSpin; ++sp) { const u64 fl = __hip_atomic_load(fp, RLX_AGENT); if (__all(fl == tag)) break; __builtin_amdgcn_s_sleep(1); }
        u32x4 of[32];
#pragma unroll
        for (int i = 0; i < 32; ++i) of[i] = __builtin_amdgcn_raw_buffer_load_b128(rWS, WS_OB + T * 32768 + lane * 16, i * 1024, 16);
        bar();
        f32x16 acc = {};
#pragma unroll
        for (int hd = 0; hd < 16; ++hd) { acc = mfma(WOF(0, hd, 0), of[2 * hd], acc); acc = mfma(WOF(0, hd, 1), of[2 * hd + 1], acc); }
#pragma unroll
        for (int i = 0; i < 4; ++i) *(LAS f32x4*)(mg + 16384 + i * 1024) = (f32x4){acc[4 * i], acc[4 * i + 1], acc[4 * i + 2], acc[4 * i + 3]};
        STAMP(9); STAMP(10);
        bar();
    } else {
        bar();
        const float mo = *(const LAS float*)(lds + MBUF + (32 * w4 + r) * 4);
        const float mm = fmaxf(mref, mo), fa = ex2(mref - mm), fb = ex2(mo - mm);
#pragma unroll
        for (int i = 0; i < 4; ++i) { const f32x4 t = *(const LAS f32x4*)(mg + i * 1024);
#pragma unroll
            for (int j = 0; j < 4; ++j) O[4 * i + j] = O[4 * i + j] * fa + t[j] * fb; }
        const float sc = gq / from_hi(O[12]);
#pragma unroll
        for (int i = 0; i < 16; ++i) O[i] *= sc;
        u32x4 f0 = pack8(O, 0), f1 = pack8(O, 1);
        if (hh) { f1[2] = 0u; f1[3] = 0u; }
#pragma unroll
        for (int i = 0; i < 4; ++i) { f0[i] = f0[i] == ~0u ? 0xFFFE7FFFu : f0[i]; f1[i] = f1[i] == ~0u ? 0xFFFE7FFFu : f1[i]; }
        { const unsigned vo = WS_OA + ((rb * 4 + w4) * 16 + h) * 2048 + lane * 16;
          __builtin_amdgcn_raw_buffer_store_b128(f0, rWS, vo, 0, 16); __builtin_amdgcn_raw_buffer_store_b128(f1, rWS, vo + 1024, 0, 16); }
        STAMP(9);
        u32x4 of[32];
        for (unsigned sp = 0; sp < kSpin; ++sp) {
#pragma unroll
            for (int i = 0; i < 32; ++i) of[i] = __builtin_amdgcn_raw_buffer_load_b128(rWS, WS_OA + T * 32768 + lane * 16, i * 1024, 16);
            unsigned mx = 0;
#pragma unroll
            for (int i = 0; i < 32; ++i) { mx = max(max(mx, of[i][0]), of[i][1]); mx = max(max(mx, of[i][2]), of[i][3]); }
            if (__all(mx != ~0u)) break;
            __builtin_amdgcn_s_sleep(1);
        }
        STAMP(10);
        f32x16 acc = {};
#pragma unroll
        for (int hd = 0; hd < 16; ++hd) { acc = mfma(WOF(0, hd, 0), of[2 * hd], acc); acc = mfma(WOF(0, hd, 1), of[2 * hd + 1], acc); }
        bar();
        GAS char* orow = (GAS char*)out + (T * 32 + r) * XROW + (h & 7) * 112 + ct * 64 + hh * 8;
#pragma unroll
        for (int i = 0; i < 4; ++i) { const f32x4 t = *(const LAS f32x4*)(mg + 16384 + i * 1024);
            if (i < 3 || ct == 0) *(GAS u32x2*)(orow + i * 16) = (u32x2){cvtpk(acc[4 * i] + t[0], acc[4 * i + 1] + t[1]), cvtpk(acc[4 * i + 2] + t[2], acc[4 * i + 3] + t[3])}; }
        if (blockIdx.x == 0 && threadIdx.x == 0) __hip_atomic_store((GAS u64*)(ws + WS_EPOCH), tag, RLX_AGENT);
    }
}

extern "C" void kernel_launch(void* const* d_in, const int* in_sizes, int n_in, void* d_out, int out_size, void* d_ws, size_t ws_size, hipStream_t stream) {
    fused_kernel<<<256, 512, 0, stream>>>((const char*)d_in[0], (const char*)d_in[1], (const char*)d_in[2], (const char*)d_in[4], (const char*)d_in[5],
                                          (const char*)d_in[6], (char*)d_out, (char*)d_ws);
}
```

```cpp
#include <hip/hip_runtime.h>
#include <hip/hip_bf16.h>
#include <stdint.h>

typedef __bf16 bf16x8_t __attribute__((ext_vector_type(8)));
typedef __bf16 bf16x2_t __attribute__((ext_vector_type(2)));
typedef float f32x2 __attribute__((ext_vector_type(2)));
typedef float f32x4 __attribute__((ext_vector_type(4)));
typedef float f32x16 __attribute__((ext_vector_type(16)));
typedef unsigned u32x4 __attribute__((ext_vector_type(4)));
typedef unsigned u32x2 __attribute__((ext_vector_type(2)));
typedef short s16x4 __attribute__((ext_vector_type(4)));
typedef unsigned long long u64;
#define LAS __attribute__((address_space(3)))
#define GAS __attribute__((address_space(1)))
#define DI __device__ __forceinline__
#define SBAR() __builtin_amdgcn_sched_barrier(0)
#define RLX_AGENT __ATOMIC_RELAXED, __HIP_MEMORY_SCOPE_AGENT
#define RLX_WG __ATOMIC_RELAXED, __HIP_MEMORY_SCOPE_WORKGROUP
#define OPQ(p) asm volatile("" : "+v"(p))

#define NOOOB 0
#define PRIO 2
constexpr int SEQ = 2048, DM = 448, N3 = 1344;
constexpr int XROW = DM * 2;
constexpr int WROW = N3 * 2;
constexpr float QSCALE = 0.18898223650461362f * 1.4426950408889634f;

constexpr unsigned WS_EPOCH = 0;
constexpr unsigned WS_FKV = 4096;
constexpr unsigned WS_FOB = WS_FKV + 16384;
constexpr unsigned WS_K = 65536;
constexpr unsigned WS_V = WS_K + 64 * 28672;
constexpr unsigned WS_OA = WS_V + 2097152;
constexpr unsigned WS_OB = WS_OA + 2097152;
constexpr unsigned WS_END = WS_OB + 2097152;
#define STAMP(i) do {} while (0)
#define CSTAMP(i) do {} while (0)

constexpr unsigned SLOT = 24576;
constexpr unsigned WQ = 98304;
constexpr unsigned XB = 114688;
constexpr unsigned HALO = 135168, HROW = 912;
constexpr unsigned QX = HALO + 16 * HROW;
constexpr unsigned GATE = QX + 8192;
constexpr unsigned GBUF = GATE + 896;
constexpr unsigned MREF = GBUF + 512;
constexpr unsigned CNT = MREF + 2048;
constexpr unsigned LDS_BYTES = 163840;
constexpr unsigned WOB = 0, WOA = 57344, MG = WOA;

DI unsigned cvtpk(float a, float b) { f32x2 v = {a, b}; return __builtin_bit_cast(unsigned, __builtin_convertvector(v, bf16x2_t)); }
DI f32x16 mfma(u32x4 a, u32x4 b, f32x16 c) { return __builtin_amdgcn_mfma_f32_32x32x16_bf16(__builtin_bit_cast(bf16x8_t, a), __builtin_bit_cast(bf16x8_t, b), c, 0, 0, 0); }
DI f32x4 mfma16(u32x4 a, u32x4 b, f32x4 c) { return __builtin_amdgcn_mfma_f32_16x16x32_bf16(__builtin_bit_cast(bf16x8_t, a), __builtin_bit_cast(bf16x8_t, b), c, 0, 0, 0); }
DI u32x4 pack8(const f32x16& a, int s) { return (u32x4){cvtpk(a[8 * s], a[8 * s + 1]), cvtpk(a[8 * s + 2], a[8 * s + 3]), cvtpk(a[8 * s + 4], a[8 * s + 5]), cvtpk(a[8 * s + 6], a[8 * s + 7])}; }
DI u32x4 pack8n(const f32x16& a, int s) {
    const u32x4 p = pack8(a, s);
    const auto x = __builtin_amdgcn_permlane32_swap(p[0], p[2], false, false), y = __builtin_amdgcn_permlane32_swap(p[1], p[3], false, false);
    return (u32x4){x[0], y[0], x[1], y[1]};
}
DI u32x4 ldsr(const LAS char* p) { return *(const LAS u32x4*)p; }
DI void ldsw(LAS char* p, u32x4 v) { *(LAS u32x4*)p = v; }
DI u32x2 trr(const LAS char* p) { return __builtin_bit_cast(u32x2, __builtin_amdgcn_ds_read_tr16_b64_v4i16((LAS s16x4*)p)); }
DI u32x4 tr2(const LAS char* p0, const LAS char* p1) { u32x2 a = trr(p0), b = trr(p1); return (u32x4){a[0], a[1], b[0], b[1]}; }
DI float ex2(float x) { return __builtin_amdgcn_exp2f(x); }
DI float swap_max(float m) { auto rr = __builtin_amdgcn_permlane32_swap(__float_as_uint(m), __float_as_uint(m), false, false); return fmaxf(__uint_as_float(rr[0]), __uint_as_float(rr[1])); }
DI float from_hi(float v) { auto rr = __builtin_amdgcn_permlane32_swap(__float_as_uint(v), __float_as_uint(v), false, false); return __uint_as_float(rr[1]); }
DI float from_lo(float v) { auto rr = __builtin_amdgcn_permlane32_swap(__float_as_uint(v), __float_as_uint(v), false, false); return __uint_as_float(rr[0]); }
DI u32x4 mksrd(const void* p, unsigned bytes) { u64 a = (u64)p; return (u32x4){(unsigned)a, (unsigned)(a >> 32) & 0xffffu, bytes, 0x00020000u}; }
DI void dma16(u32x4 srd, unsigned voff, unsigned soff, unsigned ldsaddr) {
    unsigned keep;
    asm volatile("s_mov_b32 %0, m0\n\ts_mov_b32 m0, %4\n\ts_nop 0\n\tbuffer_load_dwordx4 %1, %2, %3 offen lds\n\ts_mov_b32 m0, %0"
                 : "=&s"(keep) : "v"(voff), "s"(srd), "s"(soff), "s"(ldsaddr) : "memory");
}
template <int N> DI void wait_vm() { asm volatile("s_waitcnt vmcnt(%0)" ::"n"(N) : "memory"); }
DI void bar() { asm volatile("s_waitcnt lgkmcnt(0)\n\ts_barrier" ::: "memory"); }
constexpr unsigned kSpin = 1u << 18;
DI void cnt_add(LAS char* c, int lane) { asm volatile("s_waitcnt lgkmcnt(0)" ::: "memory"); if (lane == 0) __hip_atomic_fetch_add((LAS unsigned*)c, 1u, RLX_WG); }
DI void cnt_wait(LAS char* c, unsigned target) {
#pragma unroll 1
    for (unsigned sp = 0; sp < kSpin * 8; ++sp) { if ((unsigned)__builtin_amdgcn_readfirstlane(__hip_atomic_load((LAS unsigned*)c, RLX_WG)) >= target) break; __builtin_amdgcn_s_sleep(1); }
    asm volatile("" ::: "memory");
}
DI void poll_flags(const GAS u64* p, u64 tag) {
#pragma unroll 1
    for (unsigned sp = 0; sp < kSpin; ++sp) { if (__all(__hip_atomic_load(p, RLX_AGENT) == tag)) break; __builtin_amdgcn_s_sleep(1); }
    asm volatile("" ::: "memory");
}
#define MX3(a, b, c) __builtin_fmaxf(__builtin_fmaxf((a), (b)), (c))
DI float max16(const f32x16& p) {
    float a = MX3(p[0], p[1], p[2]), b = MX3(p[3], p[4], p[5]);
    a = MX3(a, p[6], p[7]); b = MX3(b, p[8], p[9]); a = MX3(a, p[10], p[11]); b = MX3(b, p[12], p[13]); a = MX3(a, p[14], p[15]);
    return __builtin_fmaxf(a, b);
}
#define LDB(rs, vo, so) __builtin_amdgcn_raw_buffer_load_b128(rs, vo, so, 16)
#define STB(v, rs, vo, so) __builtin_amdgcn_raw_buffer_store_b128(v, rs, vo, 0, 16)

__global__ __launch_bounds__(512, 2) void fused_kernel(const char* __restrict__ x, const char* __restrict__ wA, const char* __restrict__ woA,
                                                       const char* __restrict__ wB, const char* __restrict__ woB, const char* __restrict__ gw,
                                                       char* __restrict__ out, char* ws) {
    __shared__ __attribute__((aligned(16))) char lds_[LDS_BYTES];
    LAS char* lds = (LAS char*)lds_;
    const unsigned L0 = (unsigned)(size_t)lds;
    STAMP(0); CSTAMP(12);
    const int lane = threadIdx.x & 63, r = lane & 31, hh = lane >> 5;
    const int wave = __builtin_amdgcn_readfirstlane(threadIdx.x >> 6), w4 = wave & 3;
    const int xcd = blockIdx.x & 7, idx = blockIdx.x >> 3;
    const int h = (xcd & 3) * 4 + (idx >> 3), rb = (xcd >> 2) * 8 + (idx & 7), r0 = rb * 128;
    const int rot = ((idx & 7) + 2 * (idx >> 3)) % 7;
const unsigned kctab = (unsigned)(0x65432106543210ull >> (4 * rot));
#define KC(c) ((kctab >> (4 * (c))) & 7u)
    const int o0 = (7 * h) >> 1;
    const bool odd = h & 1;

    const u32x4 sX = mksrd(x, SEQ * XROW), sWA = mksrd(wA, DM * WROW), sWKV = mksrd(wA, h < 14 ? DM * WROW : 0);
    const __amdgpu_buffer_rsrc_t rWS = __builtin_amdgcn_make_buffer_rsrc(ws, 0, WS_END, 0x00020000);

    const unsigned vX0 = (r0 + 16 * wave + (lane >> 3)) * XROW + (((lane & 7) ^ (lane >> 4)) << 4);
    const unsigned vX1 = (r0 + 16 * wave + 8 + (lane >> 3)) * XROW + (((lane & 7) ^ (lane >> 4) ^ 4) << 4);
    const unsigned vsw = ((lane & 7) ^ ((lane >> 2) & 4)) << 4;
    const unsigned vKVW = (8 * wave + (lane >> 3)) * WROW + XROW + 128 * h + vsw;
#define LD_CH(c) do { const unsigned kc_ = KC(c), sl_ = L0 + ((c) & 3) * SLOT; dma16(sX, vX0, kc_ * 128, sl_ + wave * 2048); dma16(sX, vX1, kc_ * 128, sl_ + wave * 2048 + 1024); \
        dma16(sWKV, vKVW, kc_ * 64 * WROW, sl_ + 16384 + wave * 1024); } while (0)
    const int xrow = 32 * w4 + r, xf = (xrow >> 1) & 7;
    const LAS char* xa[4];
#pragma unroll
    for (int s = 0; s < 4; ++s) xa[s] = lds + xrow * 128 + (((2 * s + hh) ^ xf) << 4);
#define XFR(c, s) ldsr(((((c) & 3) == 3) ? xa3[s] : xa[s]) + (((c) & 3) == 3 ? SLOT : ((c) & 3) * SLOT))
    const LAS char* xa3[4];
#pragma unroll
    for (int s = 0; s < 4; ++s) { xa3[s] = xa[s] + 2 * SLOT; OPQ(xa3[s]); }
    const int q4 = (lane & 15) >> 2;
    const unsigned trl = ((lane >> 4) & 1) * 32 + (lane & 3) * 8;
    u64 tag;
#define READ_TAG() do { u64 e; asm volatile("s_load_dwordx2 %0, %1, 0x0\n\ts_waitcnt lgkmcnt(0)" : "=s"(e) : "s"(ws) : "memory"); tag = e * 0x9E3779B97F4A7C15ull + 0x632BE59BD9B4E019ull; } while (0)

    if (wave < 4) {
        LD_CH(0); LD_CH(1); LD_CH(2);
        const unsigned vOA = WS_OA + ((rb * 4 + w4) * 16 + h) * 2048 + lane * 16;
        if (threadIdx.x < 8) *(LAS unsigned*)(lds + CNT + threadIdx.x * 4) = 0u;
        STAMP(2);
        READ_TAG();
        f32x16 a0 = {}, a1 = {};
        const LAS char* wk0 = lds + 16384 + (8 * hh + q4) * 128 + trl + (q4 >> 1) * 64;
        const LAS char* wk1 = lds + 16384 + (8 * hh + q4) * 128 + trl + ((q4 >> 1) ^ 1) * 64;
#define WF(b, s) tr2((b) + (s) * 2048, (b) + (s) * 2048 + 512)
#define CMP_KV(c, KT) do { const LAS char* b0_ = wk0 + ((c) & 3) * SLOT; const LAS char* b1_ = wk1 + ((c) & 3) * SLOT; OPQ(b0_); OPQ(b1_); \
            _Pragma("unroll") for (int s = 0; s < 4; ++s) { const u32x4 xv_ = XFR(c, s), w0_ = WF(b0_, s), w1_ = WF(b1_, s); \
                if (KT) { a0 = mfma(w0_, xv_, a0); a1 = mfma(w1_, xv_, a1); } else { a0 = mfma(xv_, w0_, a0); a1 = mfma(xv_, w1_, a1); } } } while (0)
#define PH1A(KT) do { \
        wait_vm<6>(); bar(); STAMP(1); LD_CH(3); CMP_KV(0, KT); \
        wait_vm<6>(); bar(); LD_CH(4); CMP_KV(1, KT); \
        wait_vm<6>(); bar(); LD_CH(5); CMP_KV(2, KT); \
        wait_vm<6>(); bar(); LD_CH(6); CMP_KV(3, KT); \
        wait_vm<6>(); bar(); CMP_KV(4, KT); \
        wait_vm<3>(); bar(); CMP_KV(5, KT); \
        wait_vm<0>(); bar(); CMP_KV(6, KT); } while (0)
        if (h < 7) {
            PH1A(true);
            const unsigned vo = WS_K + (rb * 4 + w4) * 28672 + h * 4096 + lane * 16;
            STB(pack8n(a0, 0), rWS, vo, 0); STB(pack8n(a0, 1), rWS, vo + 1024, 0); STB(pack8n(a1, 0), rWS, vo + 2048, 0); STB(pack8n(a1, 1), rWS, vo + 3072, 0);
        } else {
            PH1A(false);
            const int f0 = 64 * (h - 7) + r, f1 = f0 + 32, h0 = f0 / 28, h1 = f1 / 28;
            const unsigned tb = (h < 14 ? WS_V : WS_END) + (rb * 4 + w4) * 2048 + hh * 512;
            const unsigned v0 = tb + h0 * 131072 + (f0 - 28 * h0) * 16, v1 = tb + h1 * 131072 + (f1 - 28 * h1) * 16;
            STB(pack8(a0, 0), rWS, v0, 0); STB(pack8(a0, 1), rWS, v0 + 1024, 0); STB(pack8(a1, 0), rWS, v1, 0); STB(pack8(a1, 1), rWS, v1 + 1024, 0);
            const int hp = (64 * (h - 7)) / 28 + (lane >> 4); const unsigned c = (lane & 3) == 0 ? 0x3F803F80u : 0u;
            const bool ok = h < 14 && 28 * hp + 27 < 64 * (h - 7) + 64;
            STB(((u32x4){c, c, c, c}), rWS, ok ? WS_V + hp * 131072 + (rb * 4 + w4) * 2048 + ((lane >> 2) & 3) * 512 + (28 + (lane & 3)) * 16 : WS_END, 0);
        }
        { const u32x4 ones = {~0u, ~0u, ~0u, ~0u}; STB(ones, rWS, vOA, 0); STB(ones, rWS, vOA + 1024, 0); }
        STAMP(3);
        wait_vm<0>(); bar();
        if (threadIdx.x == 0) __hip_atomic_store((GAS u64*)(ws + WS_FKV + (rb * 16 + h) * 64), tag, RLX_AGENT);
        STAMP(4);
        bar();
        STAMP(5);
        u32x4 q[4][2];
#pragma unroll
        for (int t = 0; t < 4; ++t) { q[t][0] = ldsr(lds + QX + t * 2048 + lane * 16); q[t][1] = ldsr(lds + QX + t * 2048 + 1024 + lane * 16); }
        const float gq = *(const LAS float*)(lds + GBUF + (32 * w4 + r) * 4);
        {   const int i = lane & 3, g = i == 0 ? (o0 >> 3) : i == 1 ? ((8 * o0 + 31) >> 6) : i == 2 ? 7 + ((28 * h) >> 6) : 7 + ((28 * h + 27) >> 6);
            poll_flags((const GAS u64*)(ws + WS_FKV + ((lane >> 2) * 16 + g) * 64), tag); }
        STAMP(6);
        const unsigned vK = WS_K + w4 * 28672 + o0 * 512 + lane * 16, vV = WS_V + h * 131072 + w4 * 2048 + lane * 16;
        u32x4 kv[4][4];
#define LD_KV(b, i) do { const unsigned g_ = (rb + (i)) & 15, sk_ = (NOOOB || (i) < 16) ? g_ * 114688u : WS_END, sv_ = (NOOOB || (i) < 16) ? g_ * 8192u : WS_END; \
            kv[b][0] = LDB(rWS, vK, sk_); kv[b][1] = LDB(rWS, vK + 1024, sk_); kv[b][2] = LDB(rWS, vV, sv_); kv[b][3] = LDB(rWS, vV + 1024, sv_); } while (0)
        const bool mE = !odd && hh, mO = odd && !hh;
#define KFIX(b) do { kv[b][0][0] = mO ? 0x3F80u : kv[b][0][0]; kv[b][1][2] = mE ? 0x3F80u : kv[b][1][2]; } while (0)
        LD_KV(0, 0); LD_KV(1, 1); LD_KV(2, 2);
        kv[3][3] = (u32x4){0u, 0u, 0u, 0u};
        KFIX(0);
#pragma unroll
        for (int t = 0; t < 4; ++t) {
            f32x16 S = mfma(kv[0][0], q[t][0], (f32x16){}); S = mfma(kv[0][1], q[t][1], S);
            const unsigned mb = cvtpk(swap_max(max16(S)), 0.f) & 0xffffu;
            if (!hh) *(LAS unsigned*)(lds + MREF + w4 * 512 + (32 * t + r) * 4) = mb << 16;
            q[t][0][0] = mO ? mb ^ 0x8000u : q[t][0][0]; q[t][1][2] = mE ? mb ^ 0x8000u : q[t][1][2];
        }
        STAMP(7); CSTAMP(13);
        f32x16 O[4] = {};
        f32x16 Sa = mfma(kv[0][0], q[0][0], (f32x16){}), Sb;
        Sa = mfma(kv[0][1], q[0][1], Sa);
        u32x4 pp1 = {0u, 0u, 0u, 0u};
#define EX4(Sx, i) do { Sx[i] = ex2(Sx[i]); Sx[i + 1] = ex2(Sx[i + 1]); Sx[i + 2] = ex2(Sx[i + 2]); Sx[i + 3] = ex2(Sx[i + 3]); } while (0)
#define SUB(Sx, b, t, Sn, bn, tn, bp, tp, EXTRA) do { u32x4 p0_; \
            SBAR(); Sn = mfma(kv[bn][0], q[tn][0], (f32x16){}); SBAR(); EX4(Sx, 0); SBAR(); Sn = mfma(kv[bn][1], q[tn][1], Sn); SBAR(); EX4(Sx, 4); SBAR(); \
            O[tp] = mfma(kv[bp][3], pp1, O[tp]); SBAR(); EXTRA; p0_ = pack8(Sx, 0); SBAR(); EX4(Sx, 8); SBAR(); O[t] = mfma(kv[b][2], p0_, O[t]); SBAR(); \
            EX4(Sx, 12); pp1 = pack8(Sx, 1); SBAR(); } while (0)
#define KT(b, i) do { \
            SUB(Sa, b, 0, Sb, b, 1, ((b) + 3) & 3, 3, LD_KV(((b) + 3) & 3, (i) + 3)); \
            SUB(Sb, b, 1, Sa, b, 2, b, 0, ); \
            SUB(Sa, b, 2, Sb, b, 3, b, 1, KFIX(((b) + 1) & 3)); \
            SUB(Sb, b, 3, Sa, ((b) + 1) & 3, 0, b, 2, ); } while (0)
#pragma unroll 1
        for (int i = 0; i < 16; i += 4) { KT(0, i); KT(1, i + 1); KT(2, i + 2); KT(3, i + 3); }
        O[3] = mfma(kv[3][3], pp1, O[3]);
        STAMP(8); CSTAMP(14);
        cnt_wait(lds + CNT + 4, 4);
#pragma unroll
        for (int t = 0; t < 4; ++t) if (t != w4) {
            LAS char* d = lds + MG + (t * 3 + ((w4 - t - 1) & 3)) * 4096 + lane * 16;
#pragma unroll
            for (int i = 0; i < 4; ++i) *(LAS f32x4*)(d + i * 1024) = (f32x4){O[t][4 * i], O[t][4 * i + 1], O[t][4 * i + 2], O[t][4 * i + 3]};
        }
        f32x16 Oo = w4 == 0 ? O[0] : w4 == 1 ? O[1] : w4 == 2 ? O[2] : O[3];
        cnt_add(lds + CNT + 8, lane); cnt_wait(lds + CNT + 8, 4);
        {   float mr[4];
#pragma unroll
            for (int j = 0; j < 4; ++j) mr[j] = *(const LAS float*)(lds + MREF + ((w4 + j) & 3) * 512 + (32 * w4 + r) * 4);
            const float mm = fmaxf(fmaxf(mr[0], mr[1]), fmaxf(mr[2], mr[3])), f0 = ex2(mr[0] - mm);
#pragma unroll
            for (int i = 0; i < 16; ++i) Oo[i] *= f0;
#pragma unroll
            for (int j = 0; j < 3; ++j) { const float f = ex2(mr[j + 1] - mm); const LAS char* s = lds + MG + (w4 * 3 + j) * 4096 + lane * 16;
#pragma unroll
                for (int i = 0; i < 4; ++i) { const f32x4 t = *(const LAS f32x4*)(s + i * 1024);
#pragma unroll
                    for (int k = 0; k < 4; ++k) Oo[4 * i + k] = __builtin_fmaf(t[k], f, Oo[4 * i + k]); } }
        }
        {   float z = 0.f;
#pragma unroll
            for (int i = 0; i < 16; ++i) z = __builtin_fmaf(Oo[i], 0.f, z);
            const bool bad = !(z == 0.f) || (hh && !(Oo[12] <= 1.0e18f));
            if (__any(bad)) {
                const u32x4 qa = ldsr(lds + QX + w4 * 2048 + lane * 16), qb = ldsr(lds + QX + w4 * 2048 + 1024 + lane * 16);
                Oo = (f32x16){}; float m = -__builtin_inff();
#pragma unroll 1
                for (int tt = 0; tt < 64; ++tt) {
                    const unsigned ko = WS_K + o0 * 512 + lane * 16, vo = WS_V + h * 131072 + lane * 16;
                    const u32x4 k0 = LDB(rWS, ko, tt * 28672), k1 = LDB(rWS, ko + 1024, tt * 28672), v0 = LDB(rWS, vo, tt * 2048), v1 = LDB(rWS, vo + 1024, tt * 2048);
                    f32x16 S = mfma(k0, qa, (f32x16){}); S = mfma(k1, qb, S);
                    const float mn = fmaxf(m, swap_max(max16(S))), al = ex2(m - mn);
                    m = mn;
#pragma unroll
                    for (int i = 0; i < 16; ++i) { S[i] = ex2(S[i] - mn); Oo[i] *= al; }
                    Oo = mfma(v0, pack8(S, 0), Oo); Oo = mfma(v1, pack8(S, 1), Oo);
                }
            }
        }
        const float sc = gq / from_hi(Oo[12]);
#pragma unroll
        for (int i = 0; i < 16; ++i) Oo[i] *= sc;
        u32x4 f0 = pack8(Oo, 0), f1 = pack8(Oo, 1);
        if (hh) { f1[2] = 0u; f1[3] = 0u; }
#pragma unroll
        for (int i = 0; i < 4; ++i) { f0[i] = f0[i] == ~0u ? 0xFFFE7FFFu : f0[i]; f1[i] = f1[i] == ~0u ? 0xFFFE7FFFu : f1[i]; }
        STB(f0, rWS, vOA, 0); STB(f1, rWS, vOA + 1024, 0);
        STAMP(9);
    } else {
        if (!(PRIO & 4)) __builtin_amdgcn_s_setprio(PRIO & 3);
        if (lane < 56) dma16(mksrd(gw, XROW), lane * 16, 0, L0 + GATE);
        LD_CH(0); LD_CH(1); LD_CH(2);
        STAMP(2);
        READ_TAG();
        const u32x4 sWB = mksrd(wB, DM * WROW);
        const unsigned vWr = (16 * w4 + (lane >> 2)) * WROW + (lane & 3) * 16;
        u32x4 xr[28];
#define GETX(c) do { _Pragma("unroll") for (int s = 0; s < 4; ++s) xr[4 * (c) + s] = XFR(c, s); } while (0)
#define LD_WB(c) do { _Pragma("unroll") for (int u = 0; u < 3; ++u) dma16(sWB, vWr, KC(c) * 64 * WROW + u * XROW + h * 56, L0 + (c) * 12288 + u * 4096 + w4 * 1024); } while (0)
        wait_vm<6>(); bar(); STAMP(1); LD_CH(3); GETX(0);
        wait_vm<6>(); bar(); LD_CH(4); GETX(1);
        wait_vm<6>(); bar(); LD_CH(5); GETX(2);
        wait_vm<6>(); bar(); LD_CH(6); GETX(3);
        wait_vm<6>(); bar();
#pragma unroll
        for (int c = 0; c < 7; ++c) dma16(sWA, vWr, KC(c) * 64 * WROW + o0 * 16, L0 + WQ + c * 4096 + w4 * 1024);
        GETX(4);
        wait_vm<10>(); bar(); GETX(5);
        wait_vm<7>(); bar();
        if (lane < 56) {
#pragma unroll
            for (int i = 0; i < 4; ++i) { const int hr = 4 * w4 + i, gr = r0 - 16 + hr; dma16(sX, lane * 16, (gr < 0 ? 0 : gr) * XROW, L0 + HALO + hr * HROW); }
        }
        LD_WB(0);
        GETX(6);
        wait_vm<7>();
#pragma unroll
        for (int i = 0; i < 2; ++i) { const int c = 4 * i + (lane >> 4);
            if (c < 7) *(LAS unsigned short*)(lds + WQ + c * 4096 + (16 * w4 + (lane & 15)) * 64 + (odd ? 0 : 56)) = *(const LAS unsigned short*)(lds + GATE + (KC(c) * 64 + 16 * w4 + (lane & 15)) * 2); }
        STAMP(3);
        bar();
        STAMP(4);
        LD_WB(1);
        const LAS char* wa = lds + (8 * hh + q4) * 64 + trl;
#define WFR(wb, s) tr2((wb) + (s) * 1024, (wb) + (s) * 1024 + 256)
        float gq;
        {   f32x16 aQ = {};
            const LAS char* wb = wa + WQ; OPQ(wb);
#pragma unroll
            for (int s = 0; s < 28; ++s) aQ = mfma(WFR(wb, s), xr[s], aQ);
            gq = 1.f / (1.f + __expf(-(odd ? from_lo(aQ[0]) : from_hi(aQ[12]))));
#pragma unroll
            for (int i = 0; i < 16; ++i) aQ[i] *= QSCALE;
            u32x4 q0 = pack8n(aQ, 0), q1 = pack8n(aQ, 1);
            if (odd && !hh) { q0[0] = 0u; q0[1] = 0u; }
            if (!odd && hh) { q1[2] = 0u; q1[3] = 0u; }
            ldsw(lds + QX + w4 * 2048 + lane * 16, q0); ldsw(lds + QX + w4 * 2048 + 1024 + lane * 16, q1);
            if (!hh) *(LAS float*)(lds + GBUF + (32 * w4 + r) * 4) = gq;
        }
        bar();
        if (PRIO & 4) __builtin_amdgcn_s_setprio(PRIO & 3);
        STAMP(5);
        unsigned hs = 0;
#define HSYNC() do { cnt_add(lds + CNT, lane); hs += 4; cnt_wait(lds + CNT, hs); } while (0)
        f32x16 aQ = {}, aK = {}, aV = {}; f32x4 aH = {};
        const LAS char* ha = lds + HALO + (lane & 15) * HROW + (lane >> 4) * 16;
        const LAS char* hw = lds + (1 + (w4 >> 1)) * 4096 + (w4 & 1) * 32 + (8 * (lane >> 4) + q4) * 64 + (lane & 3) * 8;
#define CMP_B(c) do { const LAS char* wb_ = wa + (c) * 12288; OPQ(wb_); const LAS char* hb_ = hw + (c) * 12288; OPQ(hb_); const LAS char* hx_ = ha + KC(c) * 128; \
            _Pragma("unroll") for (int s = 0; s < 4; ++s) { aQ = mfma(WFR(wb_, s), xr[4 * (c) + s], aQ); aK = mfma(WFR(wb_ + 4096, s), xr[4 * (c) + s], aK); aV = mfma(xr[4 * (c) + s], WFR(wb_ + 8192, s), aV); \
                if (s & 1) aH = mfma16(tr2(hb_ + (s >> 1) * 2048, hb_ + (s >> 1) * 2048 + 256), ldsr(hx_ + (s >> 1) * 64), aH); } } while (0)
        wait_vm<3>(); HSYNC(); LD_WB(2); CMP_B(0);
        wait_vm<3>(); HSYNC(); LD_WB(3); CMP_B(1);
        wait_vm<3>(); HSYNC(); LD_WB(4); CMP_B(2);
        wait_vm<3>(); HSYNC(); LD_WB(5); CMP_B(3);
        wait_vm<3>(); HSYNC(); LD_WB(6); CMP_B(4);
        wait_vm<3>(); HSYNC(); CMP_B(5);
        wait_vm<0>(); HSYNC(); CMP_B(6);
        STAMP(6);
        {   u32x4 k0 = pack8(aK, 0), k1 = pack8(aK, 1), v0 = pack8(aV, 0), v1 = pack8(aV, 1);
            if (hh) { k1[2] = 0u; k1[3] = 0u; }
            if (r >= 28) { const unsigned c = r == 28 ? 0x3F803F80u : 0u; v0 = (u32x4){c, c, c, c}; v1 = v0; }
            LAS char* d = lds + XB + (w4 + 1) * 4096 + lane * 16; ldsw(d, k0); ldsw(d + 1024, k1); ldsw(d + 2048, v0); ldsw(d + 3072, v1);
            const int k16 = lane & 15, g4 = lane >> 4; const bool pad = (w4 & 1) && g4 == 3;
            u32x2 pk = {cvtpk(aH[0], aH[1]), cvtpk(aH[2], aH[3])};
            if (w4 < 2) { if (pad) pk = (u32x2){0u, 0u};
                *(LAS u32x2*)(lds + XB + (w4 & 1) * 1024 + (16 + k16 + 32 * (g4 & 1)) * 16 + (g4 >> 1) * 8) = pk;
            } else { if (pad) pk = (u32x2){0x00003F80u, 0u};
                LAS unsigned short* e = (LAS unsigned short*)(lds + XB + 3072 + (16 * (w4 & 1) + 4 * g4 + 32 * ((k16 >> 2) & 1)) * 16 + (4 * (k16 >> 3) + (k16 & 3)) * 2);
                e[0] = (unsigned short)pk[0]; e[8] = (unsigned short)(pk[0] >> 16); e[16] = (unsigned short)pk[1]; e[24] = (unsigned short)(pk[1] >> 16); }
        }
#pragma unroll
        for (int i = 0; i < 16; ++i) aQ[i] *= QSCALE;
        u32x4 b0 = pack8(aQ, 0), b1 = pack8(aQ, 1);
        if (hh) { b1[2] = 0u; b1[3] = 0u; }
        HSYNC();
        const bool proj = (h & 7) < 7;
        const unsigned vO = (8 * w4 + (lane >> 3)) * XROW + (h & 7) * 128 + vsw;
        { const u32x4 sO = mksrd(woA, proj ? DM * XROW : 0);
#pragma unroll
          for (int i = 0; i < 14; ++i) dma16(sO, vO, i * 32 * XROW, L0 + WOA + i * 4096 + w4 * 1024); }
        {   const LAS char* tp = lds + XB + w4 * 4096 + lane * 16; OPQ(tp); const LAS char* to = tp + 4096;
            f32x16 A = mfma(ldsr(tp), b0, (f32x16){}); A = mfma(ldsr(tp + 1024), b1, A);
            f32x16 B = mfma(ldsr(to), b0, (f32x16){}); B = mfma(ldsr(to + 1024), b1, B);
            const u64 M = 0x5555ull << (18 + r);
            unsigned Mp = (unsigned)M >> (4 * hh); const unsigned Mo = (unsigned)(M >> 32) >> (4 * hh);
            if (rb == 0 && w4 == 0) Mp = 0u;
            const float NEG = -__builtin_inff();
#pragma unroll
            for (int i = 0; i < 16; ++i) { const int c = (i & 3) + 8 * (i >> 2); B[i] = ((Mo >> c) & 1u) ? B[i] : NEG; A[i] = (i >= 8 && ((Mp >> c) & 1u)) ? A[i] : NEG; }
            float m = max16(B);
            m = MX3(m, A[8], A[9]); m = MX3(m, A[10], A[11]); m = MX3(m, A[12], A[13]); m = MX3(m, A[14], A[15]);
            m = swap_max(m);
#pragma unroll
            for (int i = 0; i < 16; ++i) { B[i] = ex2(B[i] - m); if (i >= 8) A[i] = ex2(A[i] - m); }
            f32x16 OB = mfma(ldsr(tp + 3072), pack8(A, 1), (f32x16){});
            OB = mfma(ldsr(to + 2048), pack8(B, 0), OB); OB = mfma(ldsr(to + 3072), pack8(B, 1), OB);
            const float sc = (1.f - gq) / from_hi(OB[12]);
#pragma unroll
            for (int i = 0; i < 16; ++i) OB[i] *= sc;
            u32x4 f0 = pack8(OB, 0), f1 = pack8(OB, 1);
            if (hh) { f1[2] = 0u; f1[3] = 0u; }
            const unsigned vo = WS_OB + ((rb * 4 + w4) * 16 + h) * 2048 + lane * 16;
            STB(f0, rWS, vo, 0); STB(f1, rWS, vo + 1024, 0);
        }
        STAMP(7);
        { const u32x4 sO = mksrd(woB, proj ? DM * XROW : 0);
#pragma unroll
          for (int i = 0; i < 14; ++i) dma16(sO, vO, i * 32 * XROW, L0 + WOB + i * 4096 + w4 * 1024); }
        const int rt = w4 & 1, kh = w4 >> 1, T = rb * 4 + (h >> 3) * 2 + rt;
        const LAS char* wo0 = lds + kh * 28672 + (4 * hh + q4) * 128 + trl + (q4 >> 1) * 64;
        const LAS char* wo1 = lds + kh * 28672 + (4 * hh + q4) * 128 + trl + ((q4 >> 1) ^ 1) * 64;
        const unsigned wfix = 3072 - hh * 1024;
#define WOF(b, bx, hd, s) ((s) == 0 ? tr2((b) + (hd) * 3584, (b) + (hd) * 3584 + 1024) : tr2((b) + (hd) * 3584 + 2048, (bx) + (hd) * 3584))
        u32x4 wf[8][2][2];
        wait_vm<14>(); HSYNC();
        {   const LAS char* p0 = wo0 + WOA; const LAS char* p1 = wo1 + WOA; const LAS char* p0x = p0 + wfix; const LAS char* p1x = p1 + wfix; OPQ(p0); OPQ(p1); OPQ(p0x); OPQ(p1x);
#pragma unroll
            for (int hd = 0; hd < 8; ++hd) { wf[hd][0][0] = WOF(p0, p0x, hd, 0); wf[hd][1][0] = WOF(p0, p0x, hd, 1); wf[hd][0][1] = WOF(p1, p1x, hd, 0); wf[hd][1][1] = WOF(p1, p1x, hd, 1); }
        }
        cnt_add(lds + CNT + 4, lane);
        STAMP(8);
        wait_vm<0>();
        if (lane == 0) __hip_atomic_store((GAS u64*)(ws + WS_FOB + (rb * 16 + h) * 64 + w4 * 8), tag, RLX_AGENT);
        if (proj) {
            HSYNC();
            poll_flags((const GAS u64*)(ws + WS_FOB + (rb * 16 + 8 * kh + (lane & 7)) * 64 + (T & 3) * 8), tag);
            f32x16 c0 = {}, c1 = {};
            const unsigned vF = T * 32768 + kh * 16384 + lane * 16;
            {   const LAS char* p0x = wo0 + wfix; const LAS char* p1x = wo1 + wfix;
#pragma unroll
                for (int half = 0; half < 2; ++half) {
                    u32x4 of[8];
#pragma unroll
                    for (int i = 0; i < 8; ++i) of[i] = LDB(rWS, vF, WS_OB + (half * 8 + i) * 1024);
#pragma unroll
                    for (int i = 0; i < 8; ++i) { const int hd = half * 4 + (i >> 1), s = i & 1; c0 = mfma(WOF(wo0, p0x, hd, s), of[i], c0); c1 = mfma(WOF(wo1, p1x, hd, s), of[i], c1); }
                }
            }
            STAMP(9);
            cnt_wait(lds + CNT + 8, 4);
            STAMP(10);
            unsigned todo = 0xffu;
#pragma unroll 1
            for (unsigned sp = 0; sp < kSpin && todo; ++sp) {
                u32x4 of[16];
#pragma unroll
                for (int i = 0; i < 16; ++i) of[i] = LDB(rWS, vF, ((todo >> (i >> 1)) & 1u) ? WS_OA + i * 1024 : WS_END);
#pragma unroll
                for (int hd = 0; hd < 8; ++hd) {
                    const u32x4 a = of[2 * hd], b = of[2 * hd + 1];
                    const unsigned mx = max(max(max(a[0], a[1]), max(a[2], a[3])), max(max(b[0], b[1]), max(b[2], b[3])));
                    const unsigned ok = (unsigned)__all(mx != ~0u) | (unsigned)(sp + 1 == kSpin);
                    if ((todo >> hd) & ok & 1u) {
                        c0 = mfma(wf[hd][0][0], a, c0); c1 = mfma(wf[hd][0][1], a, c1); c0 = mfma(wf[hd][1][0], b, c0); c1 = mfma(wf[hd][1][1], b, c1);
                        todo &= ~(1u << hd);
                    }
                }
            }
            const f32x16 give = kh ? c0 : c1, keep = kh ? c1 : c0;
            { LAS char* d = lds + XB + w4 * 4096 + lane * 16;
#pragma unroll
              for (int i = 0; i < 4; ++i) *(LAS f32x4*)(d + i * 1024) = (f32x4){give[4 * i], give[4 * i + 1], give[4 * i + 2], give[4 * i + 3]}; }
            cnt_add(lds + CNT + 12 + rt * 4, lane); cnt_wait(lds + CNT + 12 + rt * 4, 2);
            f32x16 fin;
            { const LAS char* s = lds + XB + (w4 ^ 2) * 4096 + lane * 16;
#pragma unroll
              for (int i = 0; i < 4; ++i) { const f32x4 t = *(const LAS f32x4*)(s + i * 1024);
#pragma unroll
                  for (int k = 0; k < 4; ++k) fin[4 * i + k] = keep[4 * i + k] + t[k]; } }
            GAS char* orow = (GAS char*)out + (T * 32 + r) * XROW + (h & 7) * 128 + kh * 64 + hh * 16;
            *(GAS u32x4*)orow = pack8n(fin, 0); *(GAS u32x4*)(orow + 32) = pack8n(fin, 1);
            if (blockIdx.x == 0 && wave == 4) {
#pragma unroll 1
                for (int i = 0; i < 4; ++i) poll_flags((const GAS u64*)(ws + WS_FKV + (i * 64 + lane) * 64), tag);
                if (lane == 0) __hip_atomic_store((GAS u64*)(ws + WS_EPOCH), tag, RLX_AGENT);
            }
        }
    }
}

extern "C" void kernel_launch(void* const* d_in, const int* in_sizes, int n_in, void* d_out, int out_size, void* d_ws, size_t ws_size, hipStream_t stream) {
    fused_kernel<<<256, 512, 0, stream>>>((const char*)d_in[0], (const char*)d_in[1], (const char*)d_in[2], (const char*)d_in[4], (const char*)d_in[5],
                                          (const char*)d_in[6], (char*)d_out, (char*)d_ws);
}
```

```cpp
#include <hip/hip_runtime.h>
#include <hip/hip_bf16.h>
#include <stdint.h>

typedef __bf16 bf16x8_t __attribute__((ext_vector_type(8)));
typedef __bf16 bf16x2_t __attribute__((ext_vector_type(2)));
typedef float f32x2 __attribute__((ext_vector_type(2)));
typedef float f32x4 __attribute__((ext_vector_type(4)));
typedef float f32x16 __attribute__((ext_vector_type(16)));
typedef unsigned u32x4 __attribute__((ext_vector_type(4)));
typedef unsigned u32x2 __attribute__((ext_vector_type(2)));
typedef short s16x4 __attribute__((ext_vector_type(4)));
typedef unsigned long long u64;
#define LAS __attribute__((address_space(3)))
#define GAS __attribute__((address_space(1)))
#define DI __device__ __forceinline__
#define SBAR() __builtin_amdgcn_sched_barrier(0)
#define PRI(n) __builtin_amdgcn_s_setprio(n)
#define RLX_AGENT __ATOMIC_RELAXED, __HIP_MEMORY_SCOPE_AGENT
#define RLX_WG __ATOMIC_RELAXED, __HIP_MEMORY_SCOPE_WORKGROUP
#define OPQ(p) asm volatile("" : "+v"(p))

#define MAP 1
#define PSL 4
#define GXW 14
#define PFL 2
#define PD 8
#define TD 4
#define HSL 24
#define WIN 4
#define ABL 0
#define PRIO 2
constexpr int SEQ = 2048, DM = 448, N3 = 1344;
constexpr int XROW = DM * 2;
constexpr int WROW = N3 * 2;
constexpr float QSCALE = 0.18898223650461362f * 1.4426950408889634f;

constexpr unsigned WS_EPOCH = 0;
constexpr unsigned WS_FG = 4096;
constexpr unsigned WS_FOB = WS_FG + 4 * 36 * 128;
#define FGP(t, gid) ((GAS u64*)(ws + WS_FG + (((t) & 3) * 36 + (gid)) * 128 + ((t) >> 2) * 8))
constexpr unsigned KTILE = 28672;
constexpr unsigned VTILE = 29568;
constexpr unsigned WS_KA = 65536, WS_QB = WS_KA + 64 * KTILE, WS_KB = WS_QB + 64 * KTILE;
constexpr unsigned WS_VA = WS_KB + 64 * KTILE, WS_VB = WS_VA + 64 * VTILE;
constexpr unsigned WS_OA = WS_VB + 64 * VTILE;
constexpr unsigned WS_OB = WS_OA + 2097152;
constexpr unsigned WS_END = WS_OB + 2097152;
#define STAMP(i) do {} while (0)
#define CSTAMP(i) do {} while (0)

constexpr unsigned SLOT = 24576;
constexpr unsigned WQ = 5 * SLOT;
constexpr unsigned QX = WQ + 28672;
constexpr unsigned GATE = QX + 8192;
constexpr unsigned GBUF = GATE + 896;
constexpr unsigned MREF = GBUF + 512;
constexpr unsigned CNT = MREF + 2048;
constexpr unsigned LDS_BYTES = 163840;
constexpr unsigned WOB = 0, WOA = 57344, MG = 114688, MGT = 3712;
constexpr unsigned C_H = CNT, C_WOA = CNT + 4, C_MG = CNT + 8, C_PAIR = CNT + 12, C_ST = CNT + 20, C_FL = CNT + 24, C_Q = CNT + 28, C_QR = CNT + 32, C_CLR = CNT + 36, C_PUB = CNT + 40;
constexpr unsigned JUNK = CNT + 64;
static_assert(JUNK + 256 <= LDS_BYTES && MG + 12 * MGT <= GATE, "LDS map");

DI unsigned cvtpk(float a, float b) { f32x2 v = {a, b}; return __builtin_bit_cast(unsigned, __builtin_convertvector(v, bf16x2_t)); }
DI f32x16 mfma(u32x4 a, u32x4 b, f32x16 c) { return __builtin_amdgcn_mfma_f32_32x32x16_bf16(__builtin_bit_cast(bf16x8_t, a), __builtin_bit_cast(bf16x8_t, b), c, 0, 0, 0); }
DI f32x4 mfma16(u32x4 a, u32x4 b, f32x4 c) { return __builtin_amdgcn_mfma_f32_16x16x32_bf16(__builtin_bit_cast(bf16x8_t, a), __builtin_bit_cast(bf16x8_t, b), c, 0, 0, 0); }
DI u32x4 pack8(const f32x16& a, int s) { return (u32x4){cvtpk(a[8 * s], a[8 * s + 1]), cvtpk(a[8 * s + 2], a[8 * s + 3]), cvtpk(a[8 * s + 4], a[8 * s + 5]), cvtpk(a[8 * s + 6], a[8 * s + 7])}; }
DI u32x4 pack8n(const f32x16& a, int s) {
    const u32x4 p = pack8(a, s);
    const auto x = __builtin_amdgcn_permlane32_swap(p[0], p[2], false, false), y = __builtin_amdgcn_permlane32_swap(p[1], p[3], false, false);
    return (u32x4){x[0], y[0], x[1], y[1]};
}
DI u32x4 ldsr(const LAS char* p) { return *(const LAS u32x4*)p; }
DI void ldsw(LAS char* p, u32x4 v) { *(LAS u32x4*)p = v; }
DI u32x2 trr(const LAS char* p) { return __builtin_bit_cast(u32x2, __builtin_amdgcn_ds_read_tr16_b64_v4i16((LAS s16x4*)p)); }
DI u32x4 tr2(const LAS char* p0, const LAS char* p1) { u32x2 a = trr(p0), b = trr(p1); return (u32x4){a[0], a[1], b[0], b[1]}; }
DI float ex2(float x) { return __builtin_amdgcn_exp2f(x); }
DI float swap_max(float m) { auto rr = __builtin_amdgcn_permlane32_swap(__float_as_uint(m), __float_as_uint(m), false, false); return fmaxf(__uint_as_float(rr[0]), __uint_as_float(rr[1])); }
DI float from_hi(float v) { auto rr = __builtin_amdgcn_permlane32_swap(__float_as_uint(v), __float_as_uint(v), false, false); return __uint_as_float(rr[1]); }
DI float from_lo(float v) { auto rr = __builtin_amdgcn_permlane32_swap(__float_as_uint(v), __float_as_uint(v), false, false); return __uint_as_float(rr[0]); }
DI u32x4 mksrd(const void* p, unsigned bytes) { u64 a = (u64)p; return (u32x4){(unsigned)a, (unsigned)(a >> 32) & 0xffffu, bytes, 0x00020000u}; }
DI void dma16(u32x4 srd, unsigned voff, unsigned soff, unsigned ldsaddr) {
    unsigned keep;
    asm volatile("s_mov_b32 %0, m0\n\ts_mov_b32 m0, %4\n\ts_nop 2\n\tbuffer_load_dwordx4 %1, %2, %3 offen lds\n\ts_mov_b32 m0, %0"
                 : "=&s"(keep) : "v"(voff), "s"(srd), "s"(soff), "s"(ldsaddr) : "memory");
}
DI void dma16l(u32x4 srd, unsigned voff, unsigned soff, unsigned ldsaddr) {
    unsigned keep;
    asm volatile("s_mov_b32 %0, m0\n\ts_mov_b32 m0, %4\n\ts_setprio 0\n\ts_nop 1\n\tbuffer_load_dwordx4 %1, %2, %3 offen lds\n\ts_setprio 2\n\ts_mov_b32 m0, %0"
                 : "=&s"(keep) : "v"(voff), "s"(srd), "s"(soff), "s"(ldsaddr) : "memory");
}
DI void dma4(u32x4 srd, unsigned voff, unsigned soff, unsigned ldsaddr) {
    unsigned keep;
    asm volatile("s_mov_b32 %0, m0\n\ts_mov_b32 m0, %4\n\ts_nop 2\n\tbuffer_load_dword %1, %2, %3 offen lds\n\ts_mov_b32 m0, %0"
                 : "=&s"(keep) : "v"(voff), "s"(srd), "s"(soff), "s"(ldsaddr) : "memory");
}
template <int N> DI void wait_vm() { asm volatile("s_waitcnt vmcnt(%0)" ::"n"(N) : "memory"); }
DI void bar() { asm volatile("s_waitcnt lgkmcnt(0)\n\ts_barrier" ::: "memory"); }
constexpr unsigned kSpin = 1u << 18;
DI void cnt_add(LAS char* c, int lane) { asm volatile("s_waitcnt lgkmcnt(0)" ::: "memory"); if (lane == 0) __hip_atomic_fetch_add((LAS unsigned*)c, 1u, RLX_WG); }
template <int SL = 1> DI void cnt_wait(LAS char* c, unsigned target) {
#pragma unroll 1
    for (unsigned sp = 0; sp < kSpin * 8; ++sp) { if ((unsigned)__builtin_amdgcn_readfirstlane(__hip_atomic_load((LAS unsigned*)c, RLX_WG)) >= target) break; __builtin_amdgcn_s_sleep(SL); }
    asm volatile("" ::: "memory");
}
template <int SL = 1> DI void poll_flags(const GAS u64* p, u64 tag) {
#pragma unroll 1
    for (unsigned sp = 0; sp < kSpin; ++sp) { if (__all(__hip_atomic_load(p, RLX_AGENT) == tag)) break; __builtin_amdgcn_s_sleep(SL); }
    asm volatile("" ::: "memory");
}
#define MX3(a, b, c) __builtin_fmaxf(__builtin_fmaxf((a), (b)), (c))
DI float max16(const f32x16& p) {
    float a = MX3(p[0], p[1], p[2]), b = MX3(p[3], p[4], p[5]);
    a = MX3(a, p[6], p[7]); b = MX3(b, p[8], p[9]); a = MX3(a, p[10], p[11]); b = MX3(b, p[12], p[13]); a = MX3(a, p[14], p[15]);
    return __builtin_fmaxf(a, b);
}
#define LDB(rs, vo, so) __builtin_amdgcn_raw_buffer_load_b128(rs, vo, so, 16)
#define STB(v, rs, vo, so) __builtin_amdgcn_raw_buffer_store_b128(v, rs, vo, 0, 16)

__global__ __launch_bounds__(512, 2) void fused_kernel(const char* __restrict__ x, const char* __restrict__ wA, const char* __restrict__ woA,
                                                       const char* __restrict__ wB, const char* __restrict__ woB, const char* __restrict__ gw,
                                                       char* __restrict__ out, char* ws) {
    __shared__ __attribute__((aligned(16))) char lds_[LDS_BYTES];
    LAS char* lds = (LAS char*)lds_;
    const unsigned L0 = (unsigned)(size_t)lds;
    STAMP(0); CSTAMP(12);
    u64 tag_raw;
    asm volatile("s_load_dwordx2 %0, %1, 0x0" : "=s"(tag_raw) : "s"(ws) : "memory");
    const int lane = threadIdx.x & 63, r = lane & 31, hh = lane >> 5;
    const int wave = __builtin_amdgcn_readfirstlane(threadIdx.x >> 6), w4 = wave & 3;
    const int xcd = blockIdx.x & 7, idx = blockIdx.x >> 3;
    const int h = (xcd & 3) * 4 + (idx >> 3), rb = (xcd >> 2) * 8 + (idx & 7), rot = ((idx & 7) + 2 * (idx >> 3)) % 7;
    const int r0 = rb * 128, tile = rb * 4 + w4;
    const unsigned kctab = (unsigned)(0x65432106543210ull >> (4 * rot));
#define KC(c) ((kctab >> (4 * (c))) & 7u)
    const int o0 = (7 * h) >> 1;
    const bool odd = h & 1;
    const bool mE = !odd && hh, mO = odd && !hh;
    const int ga = o0 >> 3, gb = (8 * o0 + 31) >> 6, gc = (28 * h) >> 6, gd = (28 * h + 27) >> 6;

    const u32x4 sX = mksrd(x, SEQ * XROW), sWA = mksrd(wA, DM * WROW), sWB = mksrd(wB, DM * WROW);
    const __amdgpu_buffer_rsrc_t rWS = __builtin_amdgcn_make_buffer_rsrc(ws, 0, WS_END, 0x00020000);

    const int g1 = h < 14 ? h : h + 19;
    const u32x4 sW1 = h < 14 ? sWA : sWB;
    const unsigned vX0 = (r0 + 16 * wave + (lane >> 3)) * XROW + (((lane & 7) ^ (lane >> 4)) << 4);
    const unsigned vX1 = (r0 + 16 * wave + 8 + (lane >> 3)) * XROW + (((lane & 7) ^ (lane >> 4) ^ 4) << 4);
    const unsigned vsw = ((lane & 7) ^ ((lane >> 2) & 4)) << 4;
    const unsigned vW1 = (8 * wave + (lane >> 3)) * WROW + (h < 14 ? XROW + 128 * h : 128 * (h + 5)) + vsw;
#define LD_CH(c) do { const unsigned kc_ = KC(c), sl_ = L0 + ((c) % 5) * SLOT; dma16(sX, vX0, kc_ * 128, sl_ + wave * 2048); dma16(sX, vX1, kc_ * 128, sl_ + wave * 2048 + 1024); \
        dma16(sW1, vW1, kc_ * 64 * WROW, sl_ + 16384 + wave * 1024); } while (0)
#define LD_CH2(c) do { LD_CH(c); const unsigned kc_ = KC(c), sl_ = L0 + ((c) % 5) * SLOT; dma16(sX, vX0 - 64 * XROW, kc_ * 128, sl_ + w4 * 2048); dma16(sX, vX1 - 64 * XROW, kc_ * 128, sl_ + w4 * 2048 + 1024); \
        dma16(sW1, vW1 - 32 * WROW, kc_ * 64 * WROW, sl_ + 16384 + w4 * 1024); } while (0)
    const int xrow = 32 * w4 + r, xf = (xrow >> 1) & 7;
    const LAS char* xa[4]; const LAS char* xa3[4];
#pragma unroll
    for (int s = 0; s < 4; ++s) { xa[s] = lds + xrow * 128 + (((2 * s + hh) ^ xf) << 4); xa3[s] = xa[s] + 3 * SLOT; OPQ(xa3[s]); }
#define XFR(c, s) ldsr(((c) % 5 >= 3 ? xa3[s] : xa[s]) + ((c) % 5 >= 3 ? (c) % 5 - 3 : (c) % 5) * SLOT)
    const int q4 = (lane & 15) >> 2;
    const unsigned trl = ((lane >> 4) & 1) * 32 + (lane & 3) * 8;
    const LAS char* wk0 = lds + (8 * hh + q4) * 128 + trl + (q4 >> 1) * 64;
    const LAS char* wk1 = lds + (8 * hh + q4) * 128 + trl + ((q4 >> 1) ^ 1) * 64;
#define WF(b, s) tr2((b) + (s) * 2048, (b) + (s) * 2048 + 512)
#define GEMM2(off, KT) do { const LAS char* b0_ = wk0 + (off); const LAS char* b1_ = wk1 + (off); OPQ(b0_); OPQ(b1_); u32x4 w_[3][2]; \
        w_[0][0] = WF(b0_, 0); w_[0][1] = WF(b1_, 0); w_[1][0] = WF(b0_, 1); w_[1][1] = WF(b1_, 1); \
        _Pragma("unroll") for (int s = 0; s < 28; ++s) { if (s + 2 < 28) { w_[(s + 2) % 3][0] = WF(b0_, s + 2); w_[(s + 2) % 3][1] = WF(b1_, s + 2); } SBAR(); \
            if (KT) { a0 = mfma(w_[s % 3][0], xr[s], a0); a1 = mfma(w_[s % 3][1], xr[s], a1); } else { a0 = mfma(xr[s], w_[s % 3][0], a0); a1 = mfma(xr[s], w_[s % 3][1], a1); } SBAR(); } } while (0)
#define PUB_K(base, g7) do { const unsigned vo_ = (base) + tile * KTILE + (g7) * 4096 + lane * 16; \
        STB(pack8n(a0, 0), rWS, vo_, 0); STB(pack8n(a0, 1), rWS, vo_ + 1024, 0); STB(pack8n(a1, 0), rWS, vo_ + 2048, 0); STB(pack8n(a1, 1), rWS, vo_ + 3072, 0); } while (0)
#define PUB_V(base, g7) do { const unsigned tb_ = (base) + tile * VTILE, vo_ = tb_ + hh * 7168 + (64 * (g7) + r) * 16; \
        STB(pack8(a0, 0), rWS, vo_, 0); STB(pack8(a0, 1), rWS, vo_ + 14336, 0); STB(pack8(a1, 0), rWS, vo_ + 512, 0); STB(pack8(a1, 1), rWS, vo_ + 14848, 0); \
        const unsigned c_ = lane == 0 ? 0x3F803F80u : 0u; STB(((u32x4){c_, c_, c_, c_}), rWS, lane < 8 ? tb_ + 28672 + (g7) * 128 + lane * 16 : WS_END, 0); } while (0)
    const unsigned vVl0 = r < 28 ? hh * 7168 + (28 * h + r) * 16 : 28672 + gc * 128 + (r - 28) * 16, vVl1 = r < 28 ? vVl0 + 14336 : vVl0;
    u64 tag;
#define READ_TAG() do { asm volatile("s_waitcnt lgkmcnt(0)\n\ts_mov_b64 %0, %1" : "=s"(tag) : "s"(tag_raw) : "memory"); tag = tag * 0x9E3779B97F4A7C15ull + 0x632BE59BD9B4E019ull; } while (0)

    if (wave < 4) {
        LD_CH(0); LD_CH(1); LD_CH(2);
        if (PFL == 1) dma4(sWB, min(blockIdx.x * 40 + w4 * 10 + min(lane, 9), 9407u) * 128, 0, L0 + JUNK);
        else if (PFL == 2) dma4(sWB, ((idx & 7) * 56 + w4 * 14 + min(lane & 31, 13)) * WROW + (hh && (h == 7 || h >= 14) ? (h == 7 ? 16 : h + 3) : h) * 128, 0, L0 + JUNK);
        if (threadIdx.x < 16) *(LAS unsigned*)(lds + CNT + threadIdx.x * 4) = 0u;
        STAMP(2);
        READ_TAG();
        f32x16 a0 = {}, a1 = {};
        u32x4 fx[2][4], fw[2][8];
#define RD_KV(c) do { const LAS char* b0_ = wk0 + ((c) % 5) * SLOT + 16384; const LAS char* b1_ = wk1 + ((c) % 5) * SLOT + 16384; OPQ(b0_); OPQ(b1_); \
            _Pragma("unroll") for (int s = 0; s < 4; ++s) { fx[(c) & 1][s] = XFR(c, s); fw[(c) & 1][2 * s] = WF(b0_, s); fw[(c) & 1][2 * s + 1] = WF(b1_, s); } SBAR(); } while (0)
#define MM_KV(c, KT) do { _Pragma("unroll") for (int s = 0; s < 4; ++s) { \
            if (KT) { a0 = mfma(fw[(c) & 1][2 * s], fx[(c) & 1][s], a0); a1 = mfma(fw[(c) & 1][2 * s + 1], fx[(c) & 1][s], a1); } \
            else { a0 = mfma(fx[(c) & 1][s], fw[(c) & 1][2 * s], a0); a1 = mfma(fx[(c) & 1][s], fw[(c) & 1][2 * s + 1], a1); } } SBAR(); } while (0)
#define PH1A(KT) do { \
        wait_vm<6 + (PFL ? 1 : 0)>(); bar(); STAMP(1); RD_KV(0); \
        wait_vm<3 + (PFL ? 1 : 0)>(); bar(); RD_KV(1); MM_KV(0, KT); \
        wait_vm<(PFL ? 1 : 0)>(); bar(); RD_KV(2); MM_KV(1, KT); \
        bar(); RD_KV(3); MM_KV(2, KT); \
        bar(); RD_KV(4); MM_KV(3, KT); \
        bar(); RD_KV(5); MM_KV(4, KT); \
        bar(); RD_KV(6); MM_KV(5, KT); MM_KV(6, KT); } while (0)
        if (h < 7) { PH1A(true); PUB_K(WS_KA, h); }
        else { PH1A(false); PUB_V(h < 14 ? WS_VA : WS_VB, h < 14 ? h - 7 : h - 9); }
        cnt_add(lds + C_ST, lane);
        STAMP(3);
        wait_vm<0>();
        if (lane == 0) __hip_atomic_store(FGP(tile, g1), tag, RLX_AGENT);
        STAMP(4);
        PRI(3);
        const GAS u64* fp = FGP(((rb + lane) & 15) * 4 + w4, (lane >> 4) == 0 ? ga : (lane >> 4) == 1 ? gb : (lane >> 4) == 2 ? 7 + gc : 7 + gd);
        unsigned rdy = 0; u64 fl;
#define COUNT() do { u64 m_ = __ballot(fl == tag); m_ &= m_ >> 32; m_ &= m_ >> 16; rdy = (unsigned)__builtin_ctz(~(unsigned)m_ | 0x10000u); } while (0)
#define NEED(n) do { _Pragma("unroll 1") for (unsigned sp = 0; rdy < (n) && sp < kSpin; ++sp) { if (sp) __builtin_amdgcn_s_sleep(PSL); fl = __hip_atomic_load(fp, RLX_AGENT); COUNT(); } asm volatile("" ::: "memory"); } while (0)
        cnt_wait(lds + C_Q, 4);
        u32x4 q[4][2];
#pragma unroll
        for (int t = 0; t < 4; ++t) { const LAS char* p = lds + QX + ((w4 + 1 + t) & 3) * 2048 + lane * 16; q[t][0] = ldsr(p); q[t][1] = ldsr(p + 1024); }
        const float gq = *(const LAS float*)(lds + GBUF + (32 * w4 + r) * 4);
        STAMP(6);
        __builtin_amdgcn_s_sleep(PD);
        NEED(3u);
        cnt_add(lds + C_QR, lane);
        STAMP(5);
        const unsigned vK = WS_KA + w4 * KTILE + o0 * 512 + lane * 16, vV0 = WS_VA + w4 * VTILE + vVl0, vV1 = WS_VA + w4 * VTILE + vVl1;
        u32x4 kv[4][4];
#define LD_KV(b, i) do { const unsigned g_ = (unsigned)(rb + (i)) & 15u, sk_ = g_ * (4 * KTILE), sv_ = g_ * (4 * VTILE); \
            kv[b][0] = LDB(rWS, vK, sk_); kv[b][1] = LDB(rWS, vK + 1024, sk_); kv[b][2] = LDB(rWS, vV0, sv_); kv[b][3] = LDB(rWS, vV1, sv_); SBAR(); } while (0)
#define KFIX(b) do { kv[b][0][0] = mO ? 0x3F80u : kv[b][0][0]; kv[b][1][2] = mE ? 0x3F80u : kv[b][1][2]; } while (0)
        LD_KV(0, 0); fl = __hip_atomic_load(fp, RLX_AGENT); LD_KV(1, 1); LD_KV(2, 2);
        cnt_add(lds + C_FL, lane);
        kv[3][3] = (u32x4){0u, 0u, 0u, 0u};
        KFIX(0);
#pragma unroll
        for (int t = 0; t < 4; ++t) {
            f32x16 S = mfma(kv[0][0], q[t][0], (f32x16){}); S = mfma(kv[0][1], q[t][1], S);
            const unsigned mb = cvtpk(swap_max(max16(S)), 0.f) & 0xffffu;
            if (!hh) *(LAS unsigned*)(lds + MREF + ((w4 + 1 + t) & 3) * 512 + t * 128 + r * 4) = mb << 16;
            q[t][0][0] = mO ? mb ^ 0x8000u : q[t][0][0]; q[t][1][2] = mE ? mb ^ 0x8000u : q[t][1][2];
        }
        STAMP(7); CSTAMP(13);
        PRI(1);
        f32x16 O[4] = {};
        f32x16 Sa = mfma(kv[0][0], q[0][0], (f32x16){}), Sb;
        Sa = mfma(kv[0][1], q[0][1], Sa);
        u32x4 pp1 = {0u, 0u, 0u, 0u};
#define EX4(Sx, i) do { Sx[i] = ex2(Sx[i]); Sx[i + 1] = ex2(Sx[i + 1]); Sx[i + 2] = ex2(Sx[i + 2]); Sx[i + 3] = ex2(Sx[i + 3]); } while (0)
#define SUB(Sx, b, t, Sn, bn, tn, bp, tp, E0, E1, E2, E3, EV) do { u32x4 p0_; \
            SBAR(); Sn = mfma(kv[bn][0], q[tn][0], (f32x16){}); SBAR(); E0; SBAR(); EX4(Sx, 0); SBAR(); Sn = mfma(kv[bn][1], q[tn][1], Sn); SBAR(); E1; SBAR(); EX4(Sx, 4); SBAR(); \
            O[tp] = mfma(kv[bp][3], pp1, O[tp]); SBAR(); E2; SBAR(); p0_ = pack8(Sx, 0); EV; SBAR(); EX4(Sx, 8); SBAR(); O[t] = mfma(kv[b][2], p0_, O[t]); SBAR(); E3; SBAR(); \
            EX4(Sx, 12); pp1 = pack8(Sx, 1); SBAR(); } while (0)
#define WVM(n) __builtin_amdgcn_s_waitcnt(0x0F70 | ((n) & 15) | (((n) >> 4) << 14))
#define KT(b) do { \
            SUB(Sa, b, 0, Sb, b, 1, ((b) + 3) & 3, 3, gn = (gn + 1) & 15u, sk = gn * (4 * KTILE), kv[((b) + 3) & 3][0] = LDB(rWS, vK, sk), kv[((b) + 3) & 3][1] = LDB(rWS, vK + 1024, sk), ); \
            SUB(Sb, b, 1, Sa, b, 2, b, 0, sv = gn * (4 * VTILE), kv[((b) + 3) & 3][2] = LDB(rWS, vV0, sv), kv[((b) + 3) & 3][3] = LDB(rWS, vV1, sv), , ); \
            SUB(Sa, b, 2, Sb, b, 3, b, 1, , , WVM(8), , KFIX(((b) + 1) & 3)); \
            SUB(Sb, b, 3, Sa, ((b) + 1) & 3, 0, b, 2, , , , , ); } while (0)
#define KT1(b, n) do { \
            SUB(Sa, b, 0, Sb, b, 1, ((b) + 3) & 3, 3, , , , , ); \
            SUB(Sb, b, 1, Sa, b, 2, b, 0, , , , , ); \
            SUB(Sa, b, 2, Sb, b, 3, b, 1, , , , , KFIX(((b) + 1) & 3)); \
            if (rdy < 16u) { COUNT(); NEED(n); fl = __hip_atomic_load(fp, RLX_AGENT); } LD_KV(((b) + 3) & 3, (b) + 3); \
            SUB(Sb, b, 3, Sa, ((b) + 1) & 3, 0, b, 2, , , , , ); } while (0)
        KT1(0, 4u); KT1(1, 5u); KT1(2, 6u); KT1(3, 7u);
        if (rdy < 16u) { COUNT(); NEED(16u); }
        WVM(8);
        unsigned gn = (unsigned)(rb + 6) & 15u, sk, sv;
#pragma unroll 1
        for (int i = 0; i < 3; ++i) { KT(0); KT(1); KT(2); KT(3); }
        O[3] = mfma(kv[3][3], pp1, O[3]);
        STAMP(8); CSTAMP(14);
        PRI(3);
        cnt_wait(lds + C_QR, 4); cnt_wait(lds + C_CLR, 4);
#pragma unroll
        for (int t = 0; t < 3; ++t) {
            LAS char* d = lds + MG + (((w4 + 1 + t) & 3) * 3 + t) * MGT;
#pragma unroll
            for (int i = 0; i < 3; ++i) *(LAS f32x4*)(d + i * 1024 + lane * 16) = (f32x4){O[t][4 * i], O[t][4 * i + 1], O[t][4 * i + 2], O[t][4 * i + 3]};
            if (hh) *(LAS float*)(d + 3584 + r * 4) = O[t][12];
            else *(LAS f32x4*)(d + 3072 + r * 16) = (f32x4){O[t][12], O[t][13], O[t][14], O[t][15]};
        }
        cnt_add(lds + C_MG, lane); cnt_wait(lds + C_MG, 4);
        f32x16 Oo;
        bool bad;
        {   float mr[4], pl[3]; f32x4 pt[3][4];
            const LAS char* s = lds + MG + w4 * 3 * MGT;
#pragma unroll
            for (int t = 0; t < 4; ++t) mr[t] = *(const LAS float*)(lds + MREF + w4 * 512 + t * 128 + r * 4);
#pragma unroll
            for (int t = 0; t < 3; ++t) pl[t] = *(const LAS float*)(s + t * MGT + 3584 + r * 4);
#pragma unroll
            for (int t = 0; t < 3; ++t) {
#pragma unroll
                for (int i = 0; i < 3; ++i) pt[t][i] = *(const LAS f32x4*)(s + t * MGT + i * 1024 + lane * 16);
                pt[t][3] = *(const LAS f32x4*)(s + t * MGT + 3072 + r * 16); }
            const float mm = fmaxf(fmaxf(mr[0], mr[1]), fmaxf(mr[2], mr[3]));
            float f[4];
#pragma unroll
            for (int t = 0; t < 4; ++t) f[t] = ex2(mr[t] - mm);
            const float l = from_hi(O[3][12]) * f[3] + pl[0] * f[0] + pl[1] * f[1] + pl[2] * f[2], sc = gq * __builtin_amdgcn_rcpf(l);
#pragma unroll
            for (int t = 0; t < 4; ++t) f[t] *= sc;
#pragma unroll
            for (int i = 0; i < 16; ++i) Oo[i] = O[3][i] * f[3] + pt[0][i >> 2][i & 3] * f[0] + pt[1][i >> 2][i & 3] * f[1] + pt[2][i >> 2][i & 3] * f[2];
            float z = 0.f;
#pragma unroll
            for (int i = 0; i < 16; ++i) z += Oo[i];
            bad = !(l <= 1.0e18f) || !(__builtin_fabsf(z) <= 3.0e38f);
        }
        if (__any(bad)) {
            u32x4 qa = q[3][0], qb = q[3][1];
            if (mO) qa[0] = 0u;
            if (mE) qb[2] = 0u;
            Oo = (f32x16){}; float m = -__builtin_inff();
#pragma unroll 1
            for (int tt = 0; tt < 64; ++tt) {
                const unsigned ko = WS_KA + o0 * 512 + lane * 16;
                const u32x4 k0 = LDB(rWS, ko, tt * KTILE), k1 = LDB(rWS, ko + 1024, tt * KTILE), v0 = LDB(rWS, WS_VA + vVl0, tt * VTILE), v1 = LDB(rWS, WS_VA + vVl1, tt * VTILE);
                f32x16 S = mfma(k0, qa, (f32x16){}); S = mfma(k1, qb, S);
                const float mn = fmaxf(m, swap_max(max16(S))), al = ex2(m - mn);
                m = mn;
#pragma unroll
                for (int i = 0; i < 16; ++i) { S[i] = ex2(S[i] - mn); Oo[i] *= al; }
                Oo = mfma(v0, pack8(S, 0), Oo); Oo = mfma(v1, pack8(S, 1), Oo);
            }
            const float sc = gq / from_hi(Oo[12]);
#pragma unroll
            for (int i = 0; i < 16; ++i) Oo[i] *= sc;
        }
        u32x4 f0 = pack8(Oo, 0), f1 = pack8(Oo, 1);
        if (hh) { f1[2] = 0u; f1[3] = 0u; }
#pragma unroll
        for (int i = 0; i < 4; ++i) { f0[i] = min(f0[i], 0xFFFFFFFEu); f1[i] = min(f1[i], 0xFFFFFFFEu); }
        const unsigned vOA = WS_OA + (tile * 16 + h) * 2048 + lane * 16;
        STB(f0, rWS, vOA, 0); STB(f1, rWS, vOA + 1024, 0);
        cnt_add(lds + C_PUB, lane);
        STAMP(9);
    } else {
        __builtin_amdgcn_s_setprio(PRIO);
        if (lane < 56) dma16(mksrd(gw, XROW), lane * 16, 0, L0 + GATE);
        LD_CH(0); LD_CH(1); LD_CH(2);
        STAMP(2);
        READ_TAG();
        u32x4 xr[28];
#define GETX(c) do { _Pragma("unroll") for (int s = 0; s < 4; ++s) xr[4 * (c) + s] = XFR(c, s); } while (0)
        wait_vm<6>(); bar(); STAMP(1); LD_CH2(3); GETX(0);
        wait_vm<9>(); bar(); LD_CH2(4); GETX(1);
        wait_vm<12>(); bar(); LD_CH2(5); GETX(2);
        wait_vm<12>(); bar(); LD_CH2(6); GETX(3);
        wait_vm<12>(); bar();
        { const unsigned vWq = (16 * w4 + (lane >> 2)) * WROW + (lane & 3) * 16 + o0 * 16;
#pragma unroll
          for (int c = 0; c < 7; ++c) dma16(sWA, vWq, KC(c) * 64 * WROW, L0 + WQ + c * 4096 + w4 * 1024); }
        GETX(4);
        wait_vm<13>(); bar(); GETX(5);
        wait_vm<7>(); bar(); GETX(6);
        const int gx = h == 7 ? 16 : h == 14 ? 17 : h == 15 ? 18 : -1;
        wait_vm<0>();
#pragma unroll
        for (int i = 0; i < 2; ++i) { const int c = 4 * i + (lane >> 4);
            if (c < 7) *(LAS unsigned short*)(lds + WQ + c * 4096 + (16 * w4 + (lane & 15)) * 64 + (odd ? 0 : 56)) = *(const LAS unsigned short*)(lds + GATE + (KC(c) * 64 + 16 * w4 + (lane & 15)) * 2); }
        unsigned hs = 0;
#define HSYNC() do { cnt_add(lds + C_H, lane); hs += 4; cnt_wait<2>(lds + C_H, hs); } while (0)
        HSYNC();
        STAMP(3);
        const unsigned vW2 = (16 * w4 + (lane >> 3)) * WROW + vsw;
#define LD_W2(g, dst) do { _Pragma("unroll") for (int c = 0; c < 7; ++c) { dma16(sWB, vW2, KC(c) * 64 * WROW + (g) * 128, L0 + (dst) + c * 8192 + w4 * 2048); \
            dma16(sWB, vW2, (KC(c) * 64 + 8) * WROW + (g) * 128, L0 + (dst) + c * 8192 + w4 * 2048 + 1024); } } while (0)
        float gq;
        {   f32x16 aQ = {};
            const LAS char* wb = lds + WQ + (8 * hh + q4) * 64 + trl; OPQ(wb);
            u32x4 w_[4];
#pragma unroll
            for (int s = 0; s < 3; ++s) w_[s] = tr2(wb + s * 1024, wb + s * 1024 + 256);
#pragma unroll
            for (int s = 0; s < 28; ++s) { if (s + 3 < 28) w_[(s + 3) & 3] = tr2(wb + (s + 3) * 1024, wb + (s + 3) * 1024 + 256); SBAR(); aQ = mfma(w_[s & 3], xr[s], aQ); SBAR(); }
            gq = 1.f / (1.f + __expf(-(odd ? from_lo(aQ[0]) : from_hi(aQ[12]))));
#pragma unroll
            for (int i = 0; i < 16; ++i) aQ[i] *= QSCALE;
            u32x4 q0 = pack8n(aQ, 0), q1 = pack8n(aQ, 1);
            if (mO) { q0[0] = 0u; q0[1] = 0u; }
            if (mE) { q1[2] = 0u; q1[3] = 0u; }
            ldsw(lds + QX + w4 * 2048 + lane * 16, q0); ldsw(lds + QX + w4 * 2048 + 1024 + lane * 16, q1);
            if (!hh) *(LAS float*)(lds + GBUF + (32 * w4 + r) * 4) = gq;
            cnt_add(lds + C_Q, lane);
        }
        STAMP(4);
        if (ABL & 1) { cnt_add(lds + C_CLR, lane); wait_vm<0>(); goto done; }
        const bool proj = (h & 7) < 7;
        const unsigned vO = (8 * w4 + (lane >> 3)) * XROW + (h & 7) * 128 + vsw;
#define LD_WO(p, dst) do { const u32x4 sO_ = mksrd(p, proj ? DM * XROW : 0); _Pragma("unroll") for (int i = 0; i < 14; ++i) { wait_vm<WIN - 1>(); dma16l(sO_, vO, i * 32 * XROW, L0 + (dst) + i * 4096 + w4 * 1024); } } while (0)
        u32x4 pb[4];
        {   f32x16 a0 = {}, a1 = {};
            cnt_wait(lds + C_ST, 4);
            LD_W2(h, 0);
            if (gx >= 0) { LD_W2(gx, 57344); wait_vm<14>(); } else wait_vm<0>();
            HSYNC();
            if (h < 14) {
                GEMM2(0, true);
                if (h < 7) {
#pragma unroll
                    for (int i = 0; i < 16; ++i) { a0[i] *= QSCALE; a1[i] *= QSCALE; } }
                pb[0] = pack8n(a0, 0); pb[1] = pack8n(a0, 1); pb[2] = pack8n(a1, 0); pb[3] = pack8n(a1, 1);
            } else {
                GEMM2(0, false);
                pb[0] = pack8(a0, 0); pb[1] = pack8(a0, 1); pb[2] = pack8(a1, 0); pb[3] = pack8(a1, 1);
            }
            if (gx >= 0) {
                wait_vm<0>(); HSYNC();
                a0 = (f32x16){}; a1 = (f32x16){};
                GEMM2(57344, false);
            }
            STAMP(6);
            cnt_wait<2>(lds + C_FL, 4);
            if (h < 14) { const unsigned vo = (h < 7 ? WS_QB + h * 4096 : WS_KB + (h - 7) * 4096) + tile * KTILE + lane * 16;
                STB(pb[0], rWS, vo, 0); STB(pb[1], rWS, vo + 1024, 0); STB(pb[2], rWS, vo + 2048, 0); STB(pb[3], rWS, vo + 3072, 0);
            } else { const unsigned tb = WS_VB + tile * VTILE, vo = tb + hh * 7168 + (64 * (h - 14) + r) * 16;
                STB(pb[0], rWS, vo, 0); STB(pb[1], rWS, vo + 14336, 0); STB(pb[2], rWS, vo + 512, 0); STB(pb[3], rWS, vo + 14848, 0);
                const unsigned c = lane == 0 ? 0x3F803F80u : 0u; STB(((u32x4){c, c, c, c}), rWS, lane < 8 ? tb + 28672 + (h - 14) * 128 + lane * 16 : WS_END, 0); }
            if (gx >= 0) PUB_V(WS_VB, gx - 14);
        }
        cnt_add(lds + C_H, lane); hs += 4;
        { const u32x4 ones = {~0u, ~0u, ~0u, ~0u}; const unsigned vo = WS_OA + (tile * 16 + h) * 2048 + lane * 16;
          STB(ones, rWS, vo, 0); STB(ones, rWS, vo + 1024, 0); }
        wait_vm<0>();
        if (lane < 2 && (lane == 0 || gx >= 0)) __hip_atomic_store(FGP(tile, 14 + (lane ? gx : h)), tag, RLX_AGENT);
        cnt_add(lds + C_CLR, lane);
        STAMP(5);
        if (ABL & 2) goto done;
        cnt_wait<2>(lds + C_H, hs);
        LD_WO(woA, WOA);
        const int rt = w4 & 1, kh = w4 >> 1, T = rb * 4 + (h >> 3) * 2 + rt;
        const LAS char* wo0 = lds + kh * 28672 + (4 * hh + q4) * 128 + trl + (q4 >> 1) * 64;
        const LAS char* wo1 = lds + kh * 28672 + (4 * hh + q4) * 128 + trl + ((q4 >> 1) ^ 1) * 64;
        const unsigned wfix = 3072 - hh * 1024;
#define WOF(b, bx, hd, s) ((s) == 0 ? tr2((b) + (hd) * 3584, (b) + (hd) * 3584 + 1024) : tr2((b) + (hd) * 3584 + 2048, (bx) + (hd) * 3584))
        {   const int i = lane & 7, g = i == 0 ? 14 + ga : i == 1 ? 14 + gb : i == 2 ? 21 + ga : i == 3 ? 21 + gb : i == 4 ? 28 + gc : 28 + gd;
            const int tp = tile > 0 ? tile - 1 : 0;
            const int tf = (lane & 8) ? tp : tile;
            poll_flags<HSL>(FGP(tf, g), tag);
            STAMP(7);
            const unsigned vk = o0 * 512 + lane * 16;
            u32x4 b0 = LDB(rWS, vk, WS_QB + tile * KTILE), b1 = LDB(rWS, vk + 1024, WS_QB + tile * KTILE);
            const u32x4 kp0 = LDB(rWS, vk, WS_KB + tp * KTILE), kp1 = LDB(rWS, vk + 1024, WS_KB + tp * KTILE), ko0 = LDB(rWS, vk, WS_KB + tile * KTILE), ko1 = LDB(rWS, vk + 1024, WS_KB + tile * KTILE);
            const u32x4 vp1 = LDB(rWS, vVl1, WS_VB + tp * VTILE), vo0 = LDB(rWS, vVl0, WS_VB + tile * VTILE), vo1 = LDB(rWS, vVl1, WS_VB + tile * VTILE);
            if (mO) { b0[0] = 0u; b0[1] = 0u; }
            if (mE) { b1[2] = 0u; b1[3] = 0u; }
            f32x16 A = mfma(kp0, b0, (f32x16){}); A = mfma(kp1, b1, A);
            f32x16 B = mfma(ko0, b0, (f32x16){}); B = mfma(ko1, b1, B);
            const u64 M = 0x5555ull << (18 + r);
            unsigned Mp = (unsigned)M >> (4 * hh); const unsigned Mo = (unsigned)(M >> 32) >> (4 * hh);
            if (tile == 0) Mp = 0u;
#pragma unroll
            for (int i2 = 0; i2 < 16; ++i2) { const int c = (i2 & 3) + 8 * (i2 >> 2);
                const unsigned tb = (unsigned)__builtin_amdgcn_sbfe((int)Mo, c, 1); B[i2] = __uint_as_float((__float_as_uint(B[i2]) & tb) | (0xff800000u & ~tb));
                if (i2 >= 8) { const unsigned ta = (unsigned)__builtin_amdgcn_sbfe((int)Mp, c, 1); A[i2] = __uint_as_float((__float_as_uint(A[i2]) & ta) | (0xff800000u & ~ta)); } }
            float m = max16(B);
            m = MX3(m, A[8], A[9]); m = MX3(m, A[10], A[11]); m = MX3(m, A[12], A[13]); m = MX3(m, A[14], A[15]);
            m = swap_max(m);
#pragma unroll
            for (int i2 = 0; i2 < 16; ++i2) { B[i2] = ex2(B[i2] - m); if (i2 >= 8) A[i2] = ex2(A[i2] - m); }
            f32x16 OB = mfma(vp1, pack8(A, 1), (f32x16){});
            OB = mfma(vo0, pack8(B, 0), OB); OB = mfma(vo1, pack8(B, 1), OB);
            const float sc = (1.f - gq) * __builtin_amdgcn_rcpf(from_hi(OB[12]));
#pragma unroll
            for (int i2 = 0; i2 < 16; ++i2) OB[i2] *= sc;
            u32x4 f0 = pack8(OB, 0), f1 = pack8(OB, 1);
            if (hh) { f1[2] = 0u; f1[3] = 0u; }
            const unsigned vo = WS_OB + (tile * 16 + h) * 2048 + lane * 16;
            STB(f0, rWS, vo, 0); STB(f1, rWS, vo + 1024, 0);
        }
        wait_vm<0>();
        if (lane == 0) __hip_atomic_store((GAS u64*)(ws + WS_FOB + (tile * 16 + h) * 8), tag, RLX_AGENT);
        STAMP(8);
        if (ABL & 4) goto done;
        LD_WO(woB, WOB);
        u32x4 wf[8][2][2];
        HSYNC();
        {   const LAS char* p0 = wo0 + WOA; const LAS char* p1 = wo1 + WOA; const LAS char* p0x = p0 + wfix; const LAS char* p1x = p1 + wfix; OPQ(p0); OPQ(p1); OPQ(p0x); OPQ(p1x);
#pragma unroll
            for (int hd = 0; hd < 8; ++hd) { wf[hd][0][0] = WOF(p0, p0x, hd, 0); wf[hd][1][0] = WOF(p0, p0x, hd, 1); wf[hd][0][1] = WOF(p1, p1x, hd, 0); wf[hd][1][1] = WOF(p1, p1x, hd, 1); }
        }
        cnt_add(lds + C_WOA, lane);
        if (ABL & 8) { wait_vm<0>();
#pragma unroll
 for (int hd = 0; hd < 8; ++hd) asm volatile("" :: "v"(wf[hd][0][0]), "v"(wf[hd][0][1]), "v"(wf[hd][1][0]), "v"(wf[hd][1][1])); goto done; }
        if (proj) {
            wait_vm<0>(); HSYNC();
            poll_flags<HSL>((const GAS u64*)(ws + WS_FOB + (T * 16 + 8 * kh + (lane & 7)) * 8), tag);
            f32x16 c0 = {}, c1 = {};
            const unsigned vF = T * 32768 + kh * 16384 + lane * 16;
            {   const LAS char* p0x = wo0 + wfix; const LAS char* p1x = wo1 + wfix;
                u32x4 of[16];
                SBAR(); PRI(0);
#pragma unroll
                for (int i = 0; i < 16; ++i) of[i] = LDB(rWS, vF, WS_OB + i * 1024);
                SBAR(); PRI(PRIO);
#pragma unroll
                for (int i = 0; i < 16; ++i) { c0 = mfma(WOF(wo0, p0x, i >> 1, i & 1), of[i], c0); c1 = mfma(WOF(wo1, p1x, i >> 1, i & 1), of[i], c1); SBAR(); }
            }
            STAMP(9);
            cnt_wait<4>(lds + C_PUB, 4);
            __builtin_amdgcn_s_sleep(TD);
            STAMP(10);
            unsigned nx = 0;
#pragma unroll 1
            for (unsigned sp = 0; sp < kSpin && nx < 8; ++sp) {
                asm volatile("" ::: "memory");
                u32x4 of[16];
#pragma unroll
                for (int i = 0; i < 16; ++i) of[i] = LDB(rWS, vF, (unsigned)(i >> 1) >= nx ? WS_OA + i * 1024 : WS_END);
#pragma unroll
                for (int hd = 0; hd < 8; ++hd) {
                    const u32x4 a = of[2 * hd], b = of[2 * hd + 1];
                    const unsigned mx = max(max(max(a[0], a[1]), max(a[2], a[3])), max(max(b[0], b[1]), max(b[2], b[3])));
                    const unsigned ok = (unsigned)__all(mx != ~0u) | (unsigned)(sp + 1 == kSpin);
                    if ((unsigned)(nx == (unsigned)hd) & ok) {
                        c0 = mfma(wf[hd][0][0], a, c0); c1 = mfma(wf[hd][0][1], a, c1); c0 = mfma(wf[hd][1][0], b, c0); c1 = mfma(wf[hd][1][1], b, c1);
                        nx = hd + 1;
                    }
                }
            }
            const f32x16 give = kh ? c0 : c1, keep = kh ? c1 : c0;
            cnt_wait(lds + C_WOA, 4);
            { LAS char* d = lds + WOA + w4 * 4096 + lane * 16;
#pragma unroll
              for (int i = 0; i < 4; ++i) *(LAS f32x4*)(d + i * 1024) = (f32x4){give[4 * i], give[4 * i + 1], give[4 * i + 2], give[4 * i + 3]}; }
            cnt_add(lds + C_PAIR + rt * 4, lane); cnt_wait(lds + C_PAIR + rt * 4, 2);
            f32x16 fin;
            { const LAS char* s = lds + WOA + (w4 ^ 2) * 4096 + lane * 16;
#pragma unroll
              for (int i = 0; i < 4; ++i) { const f32x4 t = *(const LAS f32x4*)(s + i * 1024);
#pragma unroll
                  for (int k = 0; k < 4; ++k) fin[4 * i + k] = keep[4 * i + k] + t[k]; } }
            GAS char* orow = (GAS char*)out + (T * 32 + r) * XROW + (h & 7) * 128 + kh * 64 + hh * 16;
            *(GAS u32x4*)orow = pack8n(fin, 0); *(GAS u32x4*)(orow + 32) = pack8n(fin, 1);
            if (blockIdx.x == 0 && wave == 4) {
#pragma unroll 1
                for (int i = 0; i < 4; ++i) poll_flags(FGP((lane & 15) * 4, 14 + i * 4 + (lane >> 4)), tag);
                if (lane == 0) __hip_atomic_store((GAS u64*)(ws + WS_EPOCH), tag, RLX_AGENT);
            }
        }
    }
done:;
}

extern "C" void kernel_launch(void* const* d_in, const int* in_sizes, int n_in, void* d_out, int out_size, void* d_ws, size_t ws_size, hipStream_t stream) {
    fused_kernel<<<256, 512, 0, stream>>>((const char*)d_in[0], (const char*)d_in[1], (const char*)d_in[2], (const char*)d_in[4], (const char*)d_in[5],
                                          (const char*)d_in[6], (char*)d_out, (char*)d_ws);
}
```

```cpp
#include <hip/hip_runtime.h>
#include <hip/hip_bf16.h>
#include <stdint.h>

typedef __bf16 bf16x8_t __attribute__((ext_vector_type(8)));
typedef __bf16 bf16x2_t __attribute__((ext_vector_type(2)));
typedef float f32x2 __attribute__((ext_vector_type(2)));
typedef float f32x4 __attribute__((ext_vector_type(4)));
typedef float f32x16 __attribute__((ext_vector_type(16)));
typedef unsigned u32x4 __attribute__((ext_vector_type(4)));
typedef unsigned u32x2 __attribute__((ext_vector_type(2)));
typedef short s16x4 __attribute__((ext_vector_type(4)));
typedef unsigned long long u64;
#define LAS __attribute__((address_space(3)))
#define GAS __attribute__((address_space(1)))
#define DI __device__ __forceinline__
#define SBAR() __builtin_amdgcn_sched_barrier(0)
#define PRI(n) __builtin_amdgcn_s_setprio(n)
#define RLX_AGENT __ATOMIC_RELAXED, __HIP_MEMORY_SCOPE_AGENT
#define RLX_WG __ATOMIC_RELAXED, __HIP_MEMORY_SCOPE_WORKGROUP
#define OPQ(p) asm volatile("" : "+v"(p))

constexpr int WIN = 4;
constexpr int D1 = 7, D2 = 3;
constexpr int SEQ = 2048, DM = 448, N3 = 1344;
constexpr int XROW = DM * 2;
constexpr int WROW = N3 * 2;
constexpr float QSCALE = 0.18898223650461362f * 1.4426950408889634f;

constexpr unsigned WS_EPOCH = 0;
constexpr unsigned WS_FG = 4096;
constexpr unsigned WS_FOB = WS_FG + 4 * 42 * 128;
#define FGP(t, gid) ((GAS u64*)(ws + WS_FG + (((t) & 3) * 42 + (gid)) * 128 + ((t) >> 2) * 8))
constexpr unsigned KTILE = 28672;
constexpr unsigned VTILE = 29568;
constexpr unsigned WS_KA = 65536, WS_QB = WS_KA + 64 * KTILE, WS_KB = WS_QB + 64 * KTILE;
constexpr unsigned WS_VA = WS_KB + 64 * KTILE, WS_VB = WS_VA + 64 * VTILE;
constexpr unsigned WS_OA = WS_VB + 64 * VTILE;
constexpr unsigned WS_OB = WS_OA + 2097152;
constexpr unsigned WS_END = WS_OB + 2097152;

constexpr unsigned SLOT = 24576;
constexpr unsigned WQ = 5 * SLOT;
constexpr unsigned QX = WQ + 28672;
constexpr unsigned GATE = QX + 8192;
constexpr unsigned GBUF = GATE + 896;
constexpr unsigned MREF = GBUF + 512;
constexpr unsigned CNT = MREF + 2048;
constexpr unsigned LDS_BYTES = 163840;
constexpr unsigned W2 = 2 * SLOT;
constexpr unsigned WOB = 0, WOA = 57344, MG = 114688, MGT = 3712;
constexpr unsigned C_H = CNT, C_MG = CNT + 8, C_PAIR = CNT + 12, C_ST = CNT + 20, C_FL = CNT + 24, C_Q = CNT + 28, C_QR = CNT + 32, C_CLR = CNT + 36, C_PUB = CNT + 40;
constexpr unsigned JUNK = CNT + 64;
static_assert(JUNK + 256 <= LDS_BYTES && MG + 12 * MGT <= GATE, "LDS map");

DI unsigned cvtpk(float a, float b) { f32x2 v = {a, b}; return __builtin_bit_cast(unsigned, __builtin_convertvector(v, bf16x2_t)); }
DI f32x16 mfma(u32x4 a, u32x4 b, f32x16 c) { return __builtin_amdgcn_mfma_f32_32x32x16_bf16(__builtin_bit_cast(bf16x8_t, a), __builtin_bit_cast(bf16x8_t, b), c, 0, 0, 0); }
DI f32x4 mfma16(u32x4 a, u32x4 b, f32x4 c) { return __builtin_amdgcn_mfma_f32_16x16x32_bf16(__builtin_bit_cast(bf16x8_t, a), __builtin_bit_cast(bf16x8_t, b), c, 0, 0, 0); }
DI u32x4 pack8(const f32x16& a, int s) { return (u32x4){cvtpk(a[8 * s], a[8 * s + 1]), cvtpk(a[8 * s + 2], a[8 * s + 3]), cvtpk(a[8 * s + 4], a[8 * s + 5]), cvtpk(a[8 * s + 6], a[8 * s + 7])}; }
DI u32x4 pack8n(const f32x16& a, int s) {
    const u32x4 p = pack8(a, s);
    const auto x = __builtin_amdgcn_permlane32_swap(p[0], p[2], false, false), y = __builtin_amdgcn_permlane32_swap(p[1], p[3], false, false);
    return (u32x4){x[0], y[0], x[1], y[1]};
}
DI u32x4 ldsr(const LAS char* p) { return *(const LAS u32x4*)p; }
DI void ldsw(LAS char* p, u32x4 v) { *(LAS u32x4*)p = v; }
DI u32x2 trr(const LAS char* p) { return __builtin_bit_cast(u32x2, __builtin_amdgcn_ds_read_tr16_b64_v4i16((LAS s16x4*)p)); }
DI u32x4 tr2(const LAS char* p0, const LAS char* p1) { u32x2 a = trr(p0), b = trr(p1); return (u32x4){a[0], a[1], b[0], b[1]}; }
DI float ex2(float x) { return __builtin_amdgcn_exp2f(x); }
DI float swap_max(float m) { auto rr = __builtin_amdgcn_permlane32_swap(__float_as_uint(m), __float_as_uint(m), false, false); return fmaxf(__uint_as_float(rr[0]), __uint_as_float(rr[1])); }
DI float from_hi(float v) { auto rr = __builtin_amdgcn_permlane32_swap(__float_as_uint(v), __float_as_uint(v), false, false); return __uint_as_float(rr[1]); }
DI float from_lo(float v) { auto rr = __builtin_amdgcn_permlane32_swap(__float_as_uint(v), __float_as_uint(v), false, false); return __uint_as_float(rr[0]); }
DI u32x4 mksrd(const void* p, unsigned bytes) { u64 a = (u64)p; return (u32x4){(unsigned)a, (unsigned)(a >> 32) & 0xffffu, bytes, 0x00020000u}; }
DI void dma16(u32x4 srd, unsigned voff, unsigned soff, unsigned ldsaddr) {
    unsigned keep;
    asm volatile("s_mov_b32 %0, m0\n\ts_mov_b32 m0, %4\n\ts_nop 2\n\tbuffer_load_dwordx4 %1, %2, %3 offen lds\n\ts_mov_b32 m0, %0"
                 : "=&s"(keep) : "v"(voff), "s"(srd), "s"(soff), "s"(ldsaddr) : "memory");
}
DI void dma16l(u32x4 srd, unsigned voff, unsigned soff, unsigned ldsaddr) {
    unsigned keep;
    asm volatile("s_mov_b32 %0, m0\n\ts_mov_b32 m0, %4\n\ts_setprio 0\n\ts_nop 1\n\tbuffer_load_dwordx4 %1, %2, %3 offen lds\n\ts_setprio 2\n\ts_mov_b32 m0, %0"
                 : "=&s"(keep) : "v"(voff), "s"(srd), "s"(soff), "s"(ldsaddr) : "memory");
}
DI void dma4(u32x4 srd, unsigned voff, unsigned soff, unsigned ldsaddr) {
    unsigned keep;
    asm volatile("s_mov_b32 %0, m0\n\ts_mov_b32 m0, %4\n\ts_nop 2\n\tbuffer_load_dword %1, %2, %3 offen lds\n\ts_mov_b32 m0, %0"
                 : "=&s"(keep) : "v"(voff), "s"(srd), "s"(soff), "s"(ldsaddr) : "memory");
}
template <int N> DI void wait_vm() { asm volatile("s_waitcnt vmcnt(%0)" ::"n"(N) : "memory"); }
DI void bar() { asm volatile("s_waitcnt lgkmcnt(0)\n\ts_barrier" ::: "memory"); }
constexpr unsigned kSpin = 1u << 18;
DI void cnt_add(LAS char* c, int lane) { asm volatile("s_waitcnt lgkmcnt(0)" ::: "memory"); if (lane == 0) __hip_atomic_fetch_add((LAS unsigned*)c, 1u, RLX_WG); }
template <int SL = 1> DI void cnt_wait(LAS char* c, unsigned target) {
#pragma unroll 1
    for (unsigned sp = 0; sp < kSpin * 8; ++sp) { if (__builtin_expect((unsigned)__builtin_amdgcn_readfirstlane(__hip_atomic_load((LAS unsigned*)c, RLX_WG)) >= target, 1)) break; __builtin_amdgcn_s_sleep(SL); }
    asm volatile("" ::: "memory");
}
template <int SL = 1> DI void poll_flags(const GAS u64* p, u64 tag) {
#pragma unroll 1
    for (unsigned sp = 0; sp < kSpin; ++sp) { if (__all(__hip_atomic_load(p, RLX_AGENT) == tag)) break; __builtin_amdgcn_s_sleep(SL); }
    asm volatile("" ::: "memory");
}
#define MX3(a, b, c) __builtin_fmaxf(__builtin_fmaxf((a), (b)), (c))
DI float max16(const f32x16& p) {
    float a = MX3(p[0], p[1], p[2]), b = MX3(p[3], p[4], p[5]);
    a = MX3(a, p[6], p[7]); b = MX3(b, p[8], p[9]); a = MX3(a, p[10], p[11]); b = MX3(b, p[12], p[13]); a = MX3(a, p[14], p[15]);
    return __builtin_fmaxf(a, b);
}
#define LDB(rs, vo, so) __builtin_amdgcn_raw_buffer_load_b128(rs, vo, so, 16)
#define STB(v, rs, vo, so) __builtin_amdgcn_raw_buffer_store_b128(v, rs, vo, 0, 16)

__global__ __launch_bounds__(512, 2) void fused_kernel(const char* __restrict__ x, const char* __restrict__ wA, const char* __restrict__ woA,
                                                       const char* __restrict__ wB, const char* __restrict__ woB, const char* __restrict__ gw,
                                                       char* __restrict__ out, char* ws) {
    __shared__ __attribute__((aligned(16))) char lds_[LDS_BYTES];
    LAS char* lds = (LAS char*)lds_;
    const unsigned L0 = (unsigned)(size_t)lds;

    u64 tag_raw;
    asm volatile("s_load_dwordx2 %0, %1, 0x0" : "=s"(tag_raw) : "s"(ws) : "memory");
    const int lane = threadIdx.x & 63, r = lane & 31, hh = lane >> 5;
    const int wave = __builtin_amdgcn_readfirstlane(threadIdx.x >> 6), w4 = wave & 3;
    const int xcd = blockIdx.x & 7, idx = blockIdx.x >> 3;
    const int h = (xcd & 3) * 4 + (idx >> 3), rb = (xcd >> 2) * 8 + (idx & 7), rot = ((idx & 7) + 2 * (idx >> 3)) % 7;
    const int r0 = rb * 128, tile = rb * 4 + w4;
    const unsigned kctab = (unsigned)(0x65432106543210ull >> (4 * rot));
#define KC(c) ((kctab >> (4 * (c))) & 7u)
    const int o0 = (7 * h) >> 1;
    const bool odd = h & 1;
    const bool mE = !odd && hh, mO = odd && !hh;
    const int ga = o0 >> 3, gb = (8 * o0 + 31) >> 6, gc = (28 * h) >> 6, gd = (28 * h + 27) >> 6;

    const u32x4 sX = mksrd(x, SEQ * XROW), sWA = mksrd(wA, DM * WROW), sWB = mksrd(wB, DM * WROW);
    const __amdgpu_buffer_rsrc_t rWS = __builtin_amdgcn_make_buffer_rsrc(ws, 0, WS_END, 0x00020000);

    const u32x4 sW1 = h < 14 ? sWA : sWB;
    const unsigned vX0 = (r0 + 16 * wave + (lane >> 3)) * XROW + (((lane & 7) ^ (lane >> 4)) << 4);
    const unsigned vX1 = (r0 + 16 * wave + 8 + (lane >> 3)) * XROW + (((lane & 7) ^ (lane >> 4) ^ 4) << 4);
    const unsigned vsw = ((lane & 7) ^ ((lane >> 2) & 4)) << 4;
    const unsigned vW1 = (8 * wave + (lane >> 3)) * WROW + (h < 14 ? XROW + 128 * h : 128 * (h + 5)) + vsw;
#define LD_CH(c) do { const unsigned kc_ = KC(c), sl_ = L0 + ((c) % 5) * SLOT; dma16(sX, vX0, kc_ * 128, sl_ + wave * 2048); dma16(sX, vX1, kc_ * 128, sl_ + wave * 2048 + 1024); \
        dma16(sW1, vW1, kc_ * 64 * WROW, sl_ + 16384 + wave * 1024); } while (0)
    SBAR(); LD_CH(0); LD_CH(1); SBAR();

#define LD_CH2(c) do { LD_CH(c); const unsigned kc_ = KC(c), sl_ = L0 + ((c) % 5) * SLOT; dma16(sX, vX0 - 64 * XROW, kc_ * 128, sl_ + w4 * 2048); dma16(sX, vX1 - 64 * XROW, kc_ * 128, sl_ + w4 * 2048 + 1024); \
        dma16(sW1, vW1 - 32 * WROW, kc_ * 64 * WROW, sl_ + 16384 + w4 * 1024); } while (0)
    const int xrow = 32 * w4 + r, xf = (xrow >> 1) & 7;
    const LAS char* xa[4]; const LAS char* xa3[4];
#pragma unroll
    for (int s = 0; s < 4; ++s) { xa[s] = lds + xrow * 128 + (((2 * s + hh) ^ xf) << 4); xa3[s] = xa[s] + 3 * SLOT; OPQ(xa3[s]); }
#define XFR(c, s) ldsr(((c) % 5 >= 3 ? xa3[s] : xa[s]) + ((c) % 5 >= 3 ? (c) % 5 - 3 : (c) % 5) * SLOT)
    const int q4 = (lane & 15) >> 2;
    const unsigned trl = ((lane >> 4) & 1) * 32 + (lane & 3) * 8;
    const LAS char* wk0 = lds + (8 * hh + q4) * 128 + trl + (q4 >> 1) * 64;
    const LAS char* wk1 = lds + (8 * hh + q4) * 128 + trl + ((q4 >> 1) ^ 1) * 64;
#define WF(b, s) tr2((b) + (s) * 2048, (b) + (s) * 2048 + 512)
#define GEMM2(off, KT) do { const LAS char* b0_ = wk0 + (off); const LAS char* b1_ = wk1 + (off); OPQ(b0_); OPQ(b1_); u32x4 w_[D2 + 1][2]; \
        _Pragma("unroll") for (int s = 0; s < D2; ++s) { w_[s][0] = WF(b0_, s); w_[s][1] = WF(b1_, s); } \
        _Pragma("unroll") for (int s = 0; s < 28; ++s) { if (s + D2 < 28) { w_[(s + D2) % (D2 + 1)][0] = WF(b0_, s + D2); w_[(s + D2) % (D2 + 1)][1] = WF(b1_, s + D2); } SBAR(); \
            if (KT) { a0 = mfma(w_[s % (D2 + 1)][0], xr[s], a0); a1 = mfma(w_[s % (D2 + 1)][1], xr[s], a1); } else { a0 = mfma(xr[s], w_[s % (D2 + 1)][0], a0); a1 = mfma(xr[s], w_[s % (D2 + 1)][1], a1); } SBAR(); } } while (0)
#define GEMM1(acc, wb, KT) do { u32x4 w_[D1 + 1]; \
        _Pragma("unroll") for (int s = 0; s < D1; ++s) w_[s] = tr2((wb) + s * 1024, (wb) + s * 1024 + 256); \
        _Pragma("unroll") for (int s = 0; s < 28; ++s) { if (s + D1 < 28) w_[(s + D1) % (D1 + 1)] = tr2((wb) + (s + D1) * 1024, (wb) + (s + D1) * 1024 + 256); SBAR(); \
            acc = KT ? mfma(w_[s % (D1 + 1)], xr[s], acc) : mfma(xr[s], w_[s % (D1 + 1)], acc); SBAR(); } } while (0)
#define PUB_K(base, g7) do { const unsigned vo_ = (base) + tile * KTILE + (g7) * 4096 + lane * 16; \
        STB(pack8n(a0, 0), rWS, vo_, 0); STB(pack8n(a0, 1), rWS, vo_ + 1024, 0); STB(pack8n(a1, 0), rWS, vo_ + 2048, 0); STB(pack8n(a1, 1), rWS, vo_ + 3072, 0); } while (0)
#define PUB_V(base, g7) do { const unsigned tb_ = (base) + tile * VTILE, vo_ = tb_ + hh * 7168 + (64 * (g7) + r) * 16; \
        STB(pack8(a0, 0), rWS, vo_, 0); STB(pack8(a0, 1), rWS, vo_ + 14336, 0); STB(pack8(a1, 0), rWS, vo_ + 512, 0); STB(pack8(a1, 1), rWS, vo_ + 14848, 0); \
        const unsigned c_ = lane == 0 ? 0x3F803F80u : 0u; STB(((u32x4){c_, c_, c_, c_}), rWS, lane < 8 ? tb_ + 28672 + (g7) * 128 + lane * 16 : WS_END, 0); } while (0)
    const unsigned vVl0 = r < 28 ? hh * 7168 + (28 * h + r) * 16 : 28672 + gc * 128 + (r - 28) * 16, vVl1 = r < 28 ? vVl0 + 14336 : vVl0;
    u64 tag;
#define READ_TAG() do { asm volatile("s_waitcnt lgkmcnt(0)\n\ts_mov_b64 %0, %1" : "=s"(tag) : "s"(tag_raw) : "memory"); tag = tag * 0x9E3779B97F4A7C15ull + 0x632BE59BD9B4E019ull; } while (0)

    const int ux = h == 7 ? 4 : h == 8 ? 5 : h == 10 ? 6 : h == 12 ? 7 : h >= 14 ? h - 6 : -1;
    const unsigned vW2 = (16 * w4 + (lane >> 3)) * WROW + vsw;
#define LD_W2(g, dst) do { _Pragma("unroll") for (int c = 0; c < 7; ++c) { dma16(sWB, vW2, KC(c) * 64 * WROW + (g) * 128, L0 + (dst) + c * 8192 + w4 * 2048); \
            dma16(sWB, vW2, (KC(c) * 64 + 8) * WROW + (g) * 128, L0 + (dst) + c * 8192 + w4 * 2048 + 1024); } } while (0)
    if (wave < 4) {
        dma4(sWB, ((idx & 7) * 56 + w4 * 14 + min(lane & 31, 13)) * WROW + (hh && ux >= 0 ? 14 + (ux >> 1) : h) * 128, 0, L0 + JUNK);
        if (threadIdx.x < 16) *(LAS unsigned*)(lds + CNT + threadIdx.x * 4) = 0u;
        READ_TAG();
        f32x16 a0 = {}, a1 = {};
        u32x4 fx[2][4], fw[2][8];
#define RD_KV(c) do { const LAS char* b0_ = wk0 + ((c) % 5) * SLOT + 16384; const LAS char* b1_ = wk1 + ((c) % 5) * SLOT + 16384; OPQ(b0_); OPQ(b1_); \
            _Pragma("unroll") for (int s = 0; s < 4; ++s) { fx[(c) & 1][s] = XFR(c, s); fw[(c) & 1][2 * s] = WF(b0_, s); fw[(c) & 1][2 * s + 1] = WF(b1_, s); } SBAR(); } while (0)
#define MM_KV(c, KT) do { _Pragma("unroll") for (int s = 0; s < 4; ++s) { \
            if (KT) { a0 = mfma(fw[(c) & 1][2 * s], fx[(c) & 1][s], a0); a1 = mfma(fw[(c) & 1][2 * s + 1], fx[(c) & 1][s], a1); } \
            else { a0 = mfma(fx[(c) & 1][s], fw[(c) & 1][2 * s], a0); a1 = mfma(fx[(c) & 1][s], fw[(c) & 1][2 * s + 1], a1); } } SBAR(); } while (0)
#define PH1A(KT) do { \
        wait_vm<4>(); bar(); RD_KV(0); \
        wait_vm<1>(); bar(); RD_KV(1); MM_KV(0, KT); \
        bar(); RD_KV(2); MM_KV(1, KT); \
        bar(); RD_KV(3); MM_KV(2, KT); \
        bar(); RD_KV(4); MM_KV(3, KT); \
        bar(); RD_KV(5); MM_KV(4, KT); \
        bar(); RD_KV(6); MM_KV(5, KT); MM_KV(6, KT); } while (0)
#define RAISE1() do { cnt_add(lds + C_ST, lane); wait_vm<0>(); if (lane < (h < 14 ? 1 : 2)) __hip_atomic_store(FGP(tile, h < 14 ? h : 2 * h + 10 + lane), tag, RLX_AGENT); } while (0)
        if (h < 7) { PH1A(true); PUB_K(WS_KA, h); RAISE1(); }
        else { PH1A(false); PUB_V(h < 14 ? WS_VA : WS_VB, h < 14 ? h - 7 : h - 9); RAISE1();
        }

        PRI(3);
        const GAS u64* fp = FGP(((rb + lane) & 15) * 4 + w4, (lane >> 4) == 0 ? ga : (lane >> 4) == 1 ? gb : (lane >> 4) == 2 ? 7 + gc : 7 + gd);
        unsigned rdy = 0; u64 fl;
#define COUNT() do { u64 m_ = __ballot(fl == tag); m_ &= m_ >> 32; m_ &= m_ >> 16; rdy = (unsigned)__builtin_ctz(~(unsigned)m_ | 0x10000u); } while (0)
#define NEED(n) do { _Pragma("unroll 1") for (unsigned sp = 0; __builtin_expect(rdy < (n), 0) && sp < kSpin; ++sp) { if (sp) __builtin_amdgcn_s_sleep(4); fl = __hip_atomic_load(fp, RLX_AGENT); COUNT(); } asm volatile("" ::: "memory"); } while (0)
        cnt_wait(lds + C_Q, 4);
        u32x4 q[4][2];
#pragma unroll
        for (int t = 0; t < 4; ++t) { const LAS char* p = lds + QX + ((w4 + 1 + t) & 3) * 2048 + lane * 16; q[t][0] = ldsr(p); q[t][1] = ldsr(p + 1024); }
        const float gq = *(const LAS float*)(lds + GBUF + (32 * w4 + r) * 4);

        NEED(3u);

        const unsigned vK = WS_KA + w4 * KTILE + o0 * 512 + lane * 16, vV0 = WS_VA + w4 * VTILE + vVl0, vV1 = WS_VA + w4 * VTILE + vVl1;
        u32x4 kv[4][4];
#define LD_KV(b, i) do { const unsigned g_ = (unsigned)(rb + (i)) & 15u, sk_ = g_ * (4 * KTILE), sv_ = g_ * (4 * VTILE); \
            kv[b][0] = LDB(rWS, vK, sk_); kv[b][1] = LDB(rWS, vK + 1024, sk_); kv[b][2] = LDB(rWS, vV0, sv_); kv[b][3] = LDB(rWS, vV1, sv_); SBAR(); } while (0)
#define KFIX(b) do { kv[b][0][0] = mO ? 0x3F80u : kv[b][0][0]; kv[b][1][2] = mE ? 0x3F80u : kv[b][1][2]; } while (0)
        LD_KV(0, 0); fl = __hip_atomic_load(fp, RLX_AGENT); LD_KV(1, 1); LD_KV(2, 2);
        cnt_add(lds + C_FL, lane);
        kv[3][3] = (u32x4){0u, 0u, 0u, 0u};
        KFIX(0);
#pragma unroll
        for (int t = 0; t < 4; ++t) {
            f32x16 S = mfma(kv[0][0], q[t][0], (f32x16){}); S = mfma(kv[0][1], q[t][1], S);
            const unsigned mb = cvtpk(swap_max(max16(S)), 0.f) & 0xffffu;
            if (!hh) *(LAS unsigned*)(lds + MREF + ((w4 + 1 + t) & 3) * 512 + t * 128 + r * 4) = mb << 16;
            q[t][0][0] = mO ? mb ^ 0x8000u : q[t][0][0]; q[t][1][2] = mE ? mb ^ 0x8000u : q[t][1][2];
        }
        cnt_add(lds + C_QR, lane);

        PRI(1);
        f32x16 O[4] = {};
        f32x16 Sa = mfma(kv[0][0], q[0][0], (f32x16){}), Sb;
        Sa = mfma(kv[0][1], q[0][1], Sa);
        u32x4 pp1 = {0u, 0u, 0u, 0u};
#define EX4(Sx, i) do { Sx[i] = ex2(Sx[i]); Sx[i + 1] = ex2(Sx[i + 1]); Sx[i + 2] = ex2(Sx[i + 2]); Sx[i + 3] = ex2(Sx[i + 3]); } while (0)
#define SUB(Sx, b, t, Sn, bn, tn, bp, tp, E0, E1, E2, E3, EV) do { u32x4 p0_; \
            SBAR(); Sn = mfma(kv[bn][0], q[tn][0], (f32x16){}); SBAR(); E0; SBAR(); EX4(Sx, 0); SBAR(); Sn = mfma(kv[bn][1], q[tn][1], Sn); SBAR(); E1; SBAR(); EX4(Sx, 4); SBAR(); \
            O[tp] = mfma(kv[bp][3], pp1, O[tp]); SBAR(); E2; SBAR(); p0_ = pack8(Sx, 0); EV; SBAR(); EX4(Sx, 8); SBAR(); O[t] = mfma(kv[b][2], p0_, O[t]); SBAR(); E3; SBAR(); \
            EX4(Sx, 12); pp1 = pack8(Sx, 1); SBAR(); } while (0)
#define WVM(n) __builtin_amdgcn_s_waitcnt(0x0F70 | ((n) & 15) | (((n) >> 4) << 14))
#define KT(b) do { \
            SUB(Sa, b, 0, Sb, b, 1, ((b) + 3) & 3, 3, gn = (gn + 1) & 15u, sk = gn * (4 * KTILE), kv[((b) + 3) & 3][0] = LDB(rWS, vK, sk), kv[((b) + 3) & 3][1] = LDB(rWS, vK + 1024, sk), ); \
            SUB(Sb, b, 1, Sa, b, 2, b, 0, sv = gn * (4 * VTILE), kv[((b) + 3) & 3][2] = LDB(rWS, vV0, sv), kv[((b) + 3) & 3][3] = LDB(rWS, vV1, sv), , ); \
            SUB(Sa, b, 2, Sb, b, 3, b, 1, , , WVM(8), , KFIX(((b) + 1) & 3)); \
            SUB(Sb, b, 3, Sa, ((b) + 1) & 3, 0, b, 2, , , , , ); } while (0)
#define KT1(b, n) do { \
            SUB(Sa, b, 0, Sb, b, 1, ((b) + 3) & 3, 3, , , , , ); \
            SUB(Sb, b, 1, Sa, b, 2, b, 0, , , , , ); \
            SUB(Sa, b, 2, Sb, b, 3, b, 1, , , , , KFIX(((b) + 1) & 3)); \
            if (rdy < 16u) { COUNT(); NEED(n); fl = __hip_atomic_load(fp, RLX_AGENT); } LD_KV(((b) + 3) & 3, (b) + 3); \
            SUB(Sb, b, 3, Sa, ((b) + 1) & 3, 0, b, 2, , , , , ); } while (0)
        KT1(0, 4u); KT1(1, 5u); KT1(2, 6u); KT1(3, 7u);
        if (rdy < 16u) { COUNT(); NEED(16u); }
        WVM(8);
        unsigned gn = (unsigned)(rb + 6) & 15u, sk, sv;
        u32x2 pre = {0u, 0u};
#pragma unroll 1
        for (int i = 0; i < 3; ++i) { pre = *(volatile LAS u32x2*)(lds + C_QR); KT(0); KT(1); KT(2); KT(3); }
        O[3] = mfma(kv[3][3], pp1, O[3]);

        PRI(3);
        if (__builtin_expect(__builtin_amdgcn_readfirstlane(min(pre[0], pre[1])) < 4u, 0)) { cnt_wait(lds + C_QR, 4); cnt_wait(lds + C_CLR, 4); }
        asm volatile("" ::: "memory");
        float f[4];
#pragma unroll
        for (int t = 0; t < 4; ++t) f[t] = *(const LAS float*)(lds + MREF + w4 * 512 + t * 128 + r * 4);
#pragma unroll
        for (int t = 0; t < 3; ++t) {
            LAS char* d = lds + MG + (((w4 + 1 + t) & 3) * 3 + t) * MGT;
#pragma unroll
            for (int i = 0; i < 3; ++i) *(LAS f32x4*)(d + i * 1024 + lane * 16) = (f32x4){O[t][4 * i], O[t][4 * i + 1], O[t][4 * i + 2], O[t][4 * i + 3]};
            if (hh) *(LAS float*)(d + 3584 + r * 4) = O[t][12];
            else *(LAS f32x4*)(d + 3072 + r * 16) = (f32x4){O[t][12], O[t][13], O[t][14], O[t][15]};
        }
        cnt_add(lds + C_MG, lane);
        {   const float mm = fmaxf(fmaxf(f[0], f[1]), fmaxf(f[2], f[3]));
#pragma unroll
            for (int t = 0; t < 4; ++t) f[t] = ex2(f[t] - mm); }
        const float lo = from_hi(O[3][12]) * f[3];
        asm volatile("" :: "v"(f[0]), "v"(f[1]), "v"(f[2]), "v"(lo));
        cnt_wait(lds + C_MG, 4);
        f32x16 Oo;
        bool bad;
        {   float pl[3]; f32x4 pt[3][4];
            const LAS char* s = lds + MG + w4 * 3 * MGT;
#pragma unroll
            for (int t = 0; t < 3; ++t) pl[t] = *(const LAS float*)(s + t * MGT + 3584 + r * 4);
#pragma unroll
            for (int t = 0; t < 3; ++t) {
#pragma unroll
                for (int i = 0; i < 3; ++i) pt[t][i] = *(const LAS f32x4*)(s + t * MGT + i * 1024 + lane * 16);
                pt[t][3] = *(const LAS f32x4*)(s + t * MGT + 3072 + r * 16); }
            SBAR();
            const float l = lo + pl[0] * f[0] + pl[1] * f[1] + pl[2] * f[2], sc = gq * __builtin_amdgcn_rcpf(l);
#pragma unroll
            for (int t = 0; t < 4; ++t) f[t] *= sc;
#pragma unroll
            for (int i = 0; i < 16; ++i) Oo[i] = O[3][i] * f[3] + pt[0][i >> 2][i & 3] * f[0] + pt[1][i >> 2][i & 3] * f[1] + pt[2][i >> 2][i & 3] * f[2];
            const float z = (((Oo[0] + Oo[1]) + (Oo[2] + Oo[3])) + ((Oo[4] + Oo[5]) + (Oo[6] + Oo[7]))) + (((Oo[8] + Oo[9]) + (Oo[10] + Oo[11])) + ((Oo[12] + Oo[13]) + (Oo[14] + Oo[15])));
            bad = !(l <= 1.0e30f) || !(__builtin_fabsf(z) <= 3.0e38f);
        }
        if (__builtin_expect(__any(bad), 0)) {
            u32x4 qa = q[3][0], qb = q[3][1];
            if (mO) qa[0] = 0u;
            if (mE) qb[2] = 0u;
            Oo = (f32x16){}; float m = -__builtin_inff();
#pragma unroll 1
            for (int tt = 0; tt < 64; ++tt) {
                const unsigned ko = WS_KA + o0 * 512 + lane * 16;
                const u32x4 k0 = LDB(rWS, ko, tt * KTILE), k1 = LDB(rWS, ko + 1024, tt * KTILE), v0 = LDB(rWS, WS_VA + vVl0, tt * VTILE), v1 = LDB(rWS, WS_VA + vVl1, tt * VTILE);
                f32x16 S = mfma(k0, qa, (f32x16){}); S = mfma(k1, qb, S);
                const float mn = fmaxf(m, swap_max(max16(S))), al = ex2(m - mn);
                m = mn;
#pragma unroll
                for (int i = 0; i < 16; ++i) { S[i] = ex2(S[i] - mn); Oo[i] *= al; }
                Oo = mfma(v0, pack8(S, 0), Oo); Oo = mfma(v1, pack8(S, 1), Oo);
            }
            const float sc = gq / from_hi(Oo[12]);
#pragma unroll
            for (int i = 0; i < 16; ++i) Oo[i] *= sc;
        }
        u32x4 f0 = pack8(Oo, 0), f1 = pack8(Oo, 1);
        if (hh) { f1[2] = 0u; f1[3] = 0u; }
#pragma unroll
        for (int i = 0; i < 4; ++i) { f0[i] = min(f0[i], 0xFFFFFFFEu); f1[i] = min(f1[i], 0xFFFFFFFEu); }
        const unsigned vOA = WS_OA + (tile * 16 + h) * 2048 + lane * 16;
        STB(f0, rWS, vOA, 0); STB(f1, rWS, vOA + 1024, 0);
        cnt_add(lds + C_PUB, lane);

    } else {
        PRI(2);
        LD_CH2(2);
        READ_TAG();
        u32x4 xr[28];
#define GETX(c) do { _Pragma("unroll") for (int s = 0; s < 4; ++s) xr[4 * (c) + s] = XFR(c, s); } while (0)
        wait_vm<9>(); bar(); LD_CH2(3); GETX(0);
        wait_vm<12>(); bar(); LD_CH2(4); GETX(1);
        wait_vm<12>(); bar(); LD_CH2(5); GETX(2);
        wait_vm<12>(); bar(); LD_CH2(6); GETX(3);
        wait_vm<12>(); bar();
        if (lane < 56) dma16(mksrd(gw, XROW), lane * 16, 0, L0 + GATE);
        { const unsigned vWq = (16 * w4 + (lane >> 2)) * WROW + (lane & 3) * 16 + o0 * 16;
#pragma unroll
          for (int c = 0; c < 7; ++c) dma16(sWA, vWq, KC(c) * 64 * WROW, L0 + WQ + c * 4096 + w4 * 1024); }
        GETX(4);
        wait_vm<14>(); bar(); GETX(5);
        wait_vm<8>(); bar(); LD_W2(h, W2); GETX(6);
        wait_vm<14>();
#pragma unroll
        for (int i = 0; i < 2; ++i) { const int c = 4 * i + (lane >> 4);
            if (c < 7) *(LAS unsigned short*)(lds + WQ + c * 4096 + (16 * w4 + (lane & 15)) * 64 + (odd ? 0 : 56)) = *(const LAS unsigned short*)(lds + GATE + (KC(c) * 64 + 16 * w4 + (lane & 15)) * 2); }
        unsigned hs = 0;
#define HSYNC() do { cnt_add(lds + C_H, lane); hs += 4; cnt_wait<2>(lds + C_H, hs); } while (0)
        HSYNC();

        float gq;
        {   f32x16 aQ = {};
            const LAS char* wb = lds + WQ + (8 * hh + q4) * 64 + trl; OPQ(wb);
            GEMM1(aQ, wb, true);
            gq = 1.f / (1.f + __expf(-(odd ? from_lo(aQ[0]) : from_hi(aQ[12]))));
#pragma unroll
            for (int i = 0; i < 16; ++i) aQ[i] *= QSCALE;
            u32x4 q0 = pack8n(aQ, 0), q1 = pack8n(aQ, 1);
            if (mO) { q0[0] = 0u; q0[1] = 0u; }
            if (mE) { q1[2] = 0u; q1[3] = 0u; }
            ldsw(lds + QX + w4 * 2048 + lane * 16, q0); ldsw(lds + QX + w4 * 2048 + 1024 + lane * 16, q1);
            if (!hh) *(LAS float*)(lds + GBUF + (32 * w4 + r) * 4) = gq;
            cnt_add(lds + C_Q, lane);
        }

        const bool proj = (h & 7) < 7;
        const unsigned vO = (8 * w4 + (lane >> 3)) * XROW + (h & 7) * 128 + vsw;
#define LD_WO(p, dst) do { const u32x4 sO_ = mksrd(p, DM * XROW); _Pragma("unroll") for (int i = 0; i < 14; ++i) { wait_vm<WIN - 1>(); dma16l(sO_, vO, i * 32 * XROW, L0 + (dst) + i * 4096 + w4 * 1024); } } while (0)
        u32x4 pb[4];
#define ST_V(g7) do { const unsigned tb = WS_VB + tile * VTILE, vo = tb + hh * 7168 + (64 * (g7) + r) * 16; \
            STB(pb[0], rWS, vo, 0); STB(pb[1], rWS, vo + 14336, 0); STB(pb[2], rWS, vo + 512, 0); STB(pb[3], rWS, vo + 14848, 0); \
            const unsigned c = lane == 0 ? 0x3F803F80u : 0u; STB(((u32x4){c, c, c, c}), rWS, lane < 8 ? tb + 28672 + (g7) * 128 + lane * 16 : WS_END, 0); } while (0)
        {   f32x16 a0 = {}, a1 = {};
            if (ux >= 0) { cnt_wait(lds + C_ST, 4); const unsigned vU = (16 * w4 + (lane >> 2)) * WROW + (lane & 3) * 16 + 1792 + ux * 64;
#pragma unroll
                for (int c = 0; c < 7; ++c) dma16(sWB, vU, KC(c) * 64 * WROW, L0 + c * 4096 + w4 * 1024); }
            wait_vm<0>(); HSYNC();
            if (h < 14) {
                GEMM2(W2, true);
                if (h < 7) {
#pragma unroll
                    for (int i = 0; i < 16; ++i) { a0[i] *= QSCALE; a1[i] *= QSCALE; } }
                pb[0] = pack8n(a0, 0); pb[1] = pack8n(a0, 1); pb[2] = pack8n(a1, 0); pb[3] = pack8n(a1, 1);
            } else {
                GEMM2(W2, false);
                pb[0] = pack8(a0, 0); pb[1] = pack8(a0, 1); pb[2] = pack8(a1, 0); pb[3] = pack8(a1, 1);
            }
            if (ux >= 0) {
                a0 = (f32x16){};
                const LAS char* wb = lds + (8 * hh + q4) * 64 + trl;
                GEMM1(a0, wb, false);
            }

            cnt_wait<2>(lds + C_FL, 4);
            if (h < 14) { const unsigned vo = (h < 7 ? WS_QB + h * 4096 : WS_KB + (h - 7) * 4096) + tile * KTILE + lane * 16;
                STB(pb[0], rWS, vo, 0); STB(pb[1], rWS, vo + 1024, 0); STB(pb[2], rWS, vo + 2048, 0); STB(pb[3], rWS, vo + 3072, 0);
            } else ST_V(h - 14);
            if (ux >= 0) { const unsigned tb = WS_VB + tile * VTILE, vo = tb + hh * 7168 + (32 * ux + r) * 16;
                STB(pack8(a0, 0), rWS, vo, 0); STB(pack8(a0, 1), rWS, vo + 14336, 0);
                const unsigned c = lane == 0 ? 0x3F803F80u : 0u; STB(((u32x4){c, c, c, c}), rWS, lane < 8 ? tb + 28672 + (ux >> 1) * 128 + lane * 16 : WS_END, 0); }
        }
        cnt_add(lds + C_H, lane); hs += 4;
        { const u32x4 ones = {~0u, ~0u, ~0u, ~0u}; const unsigned vo = WS_OA + (tile * 16 + h) * 2048 + lane * 16;
          STB(ones, rWS, vo, 0); STB(ones, rWS, vo + 1024, 0); }
        wait_vm<0>();
        if (lane == 0 || (lane == 1 && h >= 14) || (lane == 2 && ux >= 0)) __hip_atomic_store(FGP(tile, lane == 2 ? 28 + ux : h < 14 ? 14 + h : 2 * h + lane), tag, RLX_AGENT);
        cnt_add(lds + C_CLR, lane);

        cnt_wait<2>(lds + C_H, hs);
        if (proj) LD_WO(woB, WOB);
        const int rt = w4 & 1, kh = w4 >> 1, T = rb * 4 + (h >> 3) * 2 + rt;
        const LAS char* wo0 = lds + kh * 28672 + (4 * hh + q4) * 128 + trl + (q4 >> 1) * 64;
        const LAS char* wo1 = lds + kh * 28672 + (4 * hh + q4) * 128 + trl + ((q4 >> 1) ^ 1) * 64;
        const unsigned wfix = 3072 - hh * 1024;
#define WOF(b, bx, hd, s) ((s) == 0 ? tr2((b) + (hd) * 3584, (b) + (hd) * 3584 + 1024) : tr2((b) + (hd) * 3584 + 2048, (bx) + (hd) * 3584))
        {   const int i = lane & 7, g = i == 0 ? 14 + ga : i == 1 ? 14 + gb : i == 2 ? 21 + ga : i == 3 ? 21 + gb : i == 4 ? 28 + ((28 * h) >> 5) : 28 + ((28 * h + 27) >> 5);
            const int tp = tile > 0 ? tile - 1 : 0;
            const int tf = (lane & 8) ? tp : tile;
            poll_flags<24>(FGP(tf, g), tag);

            const unsigned vk = o0 * 512 + lane * 16;
            u32x4 b0 = LDB(rWS, vk, WS_QB + tile * KTILE), b1 = LDB(rWS, vk + 1024, WS_QB + tile * KTILE);
            const u32x4 kp0 = LDB(rWS, vk, WS_KB + tp * KTILE), kp1 = LDB(rWS, vk + 1024, WS_KB + tp * KTILE), ko0 = LDB(rWS, vk, WS_KB + tile * KTILE), ko1 = LDB(rWS, vk + 1024, WS_KB + tile * KTILE);
            const u32x4 vp1 = LDB(rWS, vVl1, WS_VB + tp * VTILE), vo0 = LDB(rWS, vVl0, WS_VB + tile * VTILE), vo1 = LDB(rWS, vVl1, WS_VB + tile * VTILE);
            if (mO) { b0[0] = 0u; b0[1] = 0u; }
            if (mE) { b1[2] = 0u; b1[3] = 0u; }
            f32x16 A = mfma(kp0, b0, (f32x16){}); A = mfma(kp1, b1, A);
            f32x16 B = mfma(ko0, b0, (f32x16){}); B = mfma(ko1, b1, B);
            const u64 M = 0x5555ull << (18 + r);
            unsigned Mp = (unsigned)M >> (4 * hh); const unsigned Mo = (unsigned)(M >> 32) >> (4 * hh);
            if (tile == 0) Mp = 0u;
#pragma unroll
            for (int i2 = 0; i2 < 16; ++i2) { const int c = (i2 & 3) + 8 * (i2 >> 2);
                const unsigned tb = (unsigned)__builtin_amdgcn_sbfe((int)Mo, c, 1); B[i2] = __uint_as_float((__float_as_uint(B[i2]) & tb) | (0xff800000u & ~tb));
                if (i2 >= 8) { const unsigned ta = (unsigned)__builtin_amdgcn_sbfe((int)Mp, c, 1); A[i2] = __uint_as_float((__float_as_uint(A[i2]) & ta) | (0xff800000u & ~ta)); } }
            float m = max16(B);
            m = MX3(m, A[8], A[9]); m = MX3(m, A[10], A[11]); m = MX3(m, A[12], A[13]); m = MX3(m, A[14], A[15]);
            m = swap_max(m);
#pragma unroll
            for (int i2 = 0; i2 < 16; ++i2) { B[i2] = ex2(B[i2] - m); if (i2 >= 8) A[i2] = ex2(A[i2] - m); }
            f32x16 OB = mfma(vp1, pack8(A, 1), (f32x16){});
            OB = mfma(vo0, pack8(B, 0), OB); OB = mfma(vo1, pack8(B, 1), OB);
            const float sc = (1.f - gq) * __builtin_amdgcn_rcpf(from_hi(OB[12]));
#pragma unroll
            for (int i2 = 0; i2 < 16; ++i2) OB[i2] *= sc;
            u32x4 f0 = pack8(OB, 0), f1 = pack8(OB, 1);
            if (hh) { f1[2] = 0u; f1[3] = 0u; }
            const unsigned vo = WS_OB + (tile * 16 + h) * 2048 + lane * 16;
            STB(f0, rWS, vo, 0); STB(f1, rWS, vo + 1024, 0);
        }
        wait_vm<0>();
        if (lane == 0) __hip_atomic_store((GAS u64*)(ws + WS_FOB + (tile * 16 + h) * 8), tag, RLX_AGENT);

        if (proj) {
            cnt_add(lds + C_H, lane); hs += 4;
            LD_WO(woA, WOA);
            poll_flags<24>((const GAS u64*)(ws + WS_FOB + (T * 16 + 8 * kh + (lane & 7)) * 8), tag);
            cnt_wait<2>(lds + C_H, hs);
            f32x16 c0 = {}, c1 = {};
            const unsigned vF = T * 32768 + kh * 16384 + lane * 16;
            {   const LAS char* p0x = wo0 + wfix; const LAS char* p1x = wo1 + wfix;
                u32x4 of[16];
                SBAR(); PRI(0);
#pragma unroll
                for (int i = 0; i < 16; ++i) of[i] = LDB(rWS, vF, WS_OB + i * 1024);
                SBAR(); PRI(2);
#pragma unroll
                for (int i = 0; i < 16; ++i) { c0 = mfma(WOF(wo0, p0x, i >> 1, i & 1), of[i], c0); c1 = mfma(WOF(wo1, p1x, i >> 1, i & 1), of[i], c1); SBAR(); }
            }
            wait_vm<0>(); HSYNC();
            u32x4 wf[8][2][2];
            {   const LAS char* p0 = wo0 + WOA; const LAS char* p1 = wo1 + WOA; const LAS char* p0x = p0 + wfix; const LAS char* p1x = p1 + wfix; OPQ(p0); OPQ(p1); OPQ(p0x); OPQ(p1x);
#pragma unroll
                for (int hd = 0; hd < 8; ++hd) { wf[hd][0][0] = WOF(p0, p0x, hd, 0); wf[hd][1][0] = WOF(p0, p0x, hd, 1); wf[hd][0][1] = WOF(p1, p1x, hd, 0); wf[hd][1][1] = WOF(p1, p1x, hd, 1); }
            }

            cnt_wait<4>(lds + C_PUB, 4);
            __builtin_amdgcn_s_sleep(8);

            unsigned nx = 0;
#pragma unroll 1
            for (unsigned sp = 0; sp < kSpin && nx < 8; ++sp) {
                asm volatile("" ::: "memory");
                u32x4 of[16];
#pragma unroll
                for (int i = 0; i < 16; ++i) of[i] = LDB(rWS, vF, (unsigned)(i >> 1) >= nx ? WS_OA + i * 1024 : WS_END);
#pragma unroll
                for (int hd = 0; hd < 8; ++hd) {
                    const u32x4 a = of[2 * hd], b = of[2 * hd + 1];
                    const unsigned mx = max(max(max(a[0], a[1]), max(a[2], a[3])), max(max(b[0], b[1]), max(b[2], b[3])));
                    const unsigned ok = (unsigned)__all(mx != ~0u) | (unsigned)(sp + 1 == kSpin);
                    if ((unsigned)(nx == (unsigned)hd) & ok) {
                        c0 = mfma(wf[hd][0][0], a, c0); c1 = mfma(wf[hd][0][1], a, c1); c0 = mfma(wf[hd][1][0], b, c0); c1 = mfma(wf[hd][1][1], b, c1);
                        nx = hd + 1;
                    }
                }
            }
            const f32x16 give = kh ? c0 : c1, keep = kh ? c1 : c0;
            { LAS char* d = lds + WOB + w4 * 4096 + lane * 16;
#pragma unroll
              for (int i = 0; i < 4; ++i) *(LAS f32x4*)(d + i * 1024) = (f32x4){give[4 * i], give[4 * i + 1], give[4 * i + 2], give[4 * i + 3]}; }
            cnt_add(lds + C_PAIR + rt * 4, lane); cnt_wait(lds + C_PAIR + rt * 4, 2);
            f32x16 fin;
            { const LAS char* s = lds + WOB + (w4 ^ 2) * 4096 + lane * 16;
#pragma unroll
              for (int i = 0; i < 4; ++i) { const f32x4 t = *(const LAS f32x4*)(s + i * 1024);
#pragma unroll
                  for (int k = 0; k < 4; ++k) fin[4 * i + k] = keep[4 * i + k] + t[k]; } }
            GAS char* orow = (GAS char*)out + (T * 32 + r) * XROW + (h & 7) * 128 + kh * 64 + hh * 16;
            *(GAS u32x4*)orow = pack8n(fin, 0); *(GAS u32x4*)(orow + 32) = pack8n(fin, 1);
        } else if (h == 7 && rb == 0 && wave == 4) {
#pragma unroll 1
            for (int i = 0; i < 4; ++i) { const int hq = i * 4 + (lane >> 4); poll_flags<24>(FGP((lane & 15) * 4, hq < 15 ? 14 + hq : 30), tag); }
            if (lane == 0) __hip_atomic_store((GAS u64*)(ws + WS_EPOCH), tag, RLX_AGENT);
        }
    }
}

extern "C" void kernel_launch(void* const* d_in, const int* in_sizes, int n_in, void* d_out, int out_size, void* d_ws, size_t ws_size, hipStream_t stream) {
    fused_kernel<<<256, 512, 0, stream>>>((const char*)d_in[0], (const char*)d_in[1], (const char*)d_in[2], (const char*)d_in[4], (const char*)d_in[5],
                                          (const char*)d_in[6], (char*)d_out, (char*)d_ws);
}
```

```cpp
#include <hip/hip_runtime.h>
#include <hip/hip_bf16.h>
#include <stdint.h>

typedef __bf16 bf16x8_t __attribute__((ext_vector_type(8)));
typedef __bf16 bf16x2_t __attribute__((ext_vector_type(2)));
typedef float f32x2 __attribute__((ext_vector_type(2)));
typedef float f32x4 __attribute__((ext_vector_type(4)));
typedef float f32x16 __attribute__((ext_vector_type(16)));
typedef unsigned u32x4 __attribute__((ext_vector_type(4)));
typedef unsigned u32x2 __attribute__((ext_vector_type(2)));
typedef short s16x4 __attribute__((ext_vector_type(4)));
typedef unsigned long long u64;
#define LAS __attribute__((address_space(3)))
#define GAS __attribute__((address_space(1)))
#define DI __device__ __forceinline__
#define SBAR() __builtin_amdgcn_sched_barrier(0)
#define PRI(n) __builtin_amdgcn_s_setprio(n)
#define RLX_AGENT __ATOMIC_RELAXED, __HIP_MEMORY_SCOPE_AGENT
#define RLX_WG __ATOMIC_RELAXED, __HIP_MEMORY_SCOPE_WORKGROUP
#define OPQ(p) asm volatile("" : "+v"(p))

constexpr int WIN = 4;
constexpr int D1 = 7, D2 = 3;
constexpr int SEQ = 2048, DM = 448, N3 = 1344;
constexpr int XROW = DM * 2;
constexpr int WROW = N3 * 2;
constexpr float QSCALE = 0.18898223650461362f * 1.4426950408889634f;

constexpr unsigned WS_EPOCH = 0;
constexpr unsigned WS_FG = 4096;
constexpr unsigned WS_FOB = WS_FG + 4 * 42 * 128;
#define FGP(t, gid) ((GAS u64*)(ws + WS_FG + (((t) & 3) * 42 + (gid)) * 128 + ((t) >> 2) * 8))
constexpr unsigned KTILE = 28672;
constexpr unsigned VTILE = 29568;
constexpr unsigned WS_KA = 65536, WS_QB = WS_KA + 64 * KTILE, WS_KB = WS_QB + 64 * KTILE;
constexpr unsigned WS_VA = WS_KB + 64 * KTILE, WS_VB = WS_VA + 64 * VTILE;
constexpr unsigned WS_OA = WS_VB + 64 * VTILE;
constexpr unsigned WS_OB = WS_OA + 2097152;
constexpr unsigned WS_END = WS_OB + 2097152;
#define STAMP(i) do {} while (0)
#define CSTAMP(i) do {} while (0)

constexpr unsigned SLOT = 24576;
constexpr unsigned WQ = 5 * SLOT;
constexpr unsigned QX = WQ + 28672;
constexpr unsigned GATE = QX + 8192;
constexpr unsigned GBUF = GATE + 896;
constexpr unsigned MREF = GBUF + 512;
constexpr unsigned CNT = MREF + 2048;
constexpr unsigned LDS_BYTES = 163840;
constexpr unsigned W2 = 2 * SLOT;
constexpr unsigned WOB = 0, WOA = 57344, MG = 114688, MGT = 3712;
constexpr unsigned C_H = CNT, C_MG = CNT + 8, C_PAIR = CNT + 12, C_ST = CNT + 20, C_FL = CNT + 24, C_Q = CNT + 28, C_QR = CNT + 32, C_CLR = CNT + 36, C_PUB = CNT + 40;
constexpr unsigned JUNK = CNT + 64;
static_assert(JUNK + 256 <= LDS_BYTES && MG + 12 * MGT <= GATE, "LDS map");

DI unsigned cvtpk(float a, float b) { f32x2 v = {a, b}; return __builtin_bit_cast(unsigned, __builtin_convertvector(v, bf16x2_t)); }
DI f32x16 mfma(u32x4 a, u32x4 b, f32x16 c) { return __builtin_amdgcn_mfma_f32_32x32x16_bf16(__builtin_bit_cast(bf16x8_t, a), __builtin_bit_cast(bf16x8_t, b), c, 0, 0, 0); }
DI f32x4 mfma16(u32x4 a, u32x4 b, f32x4 c) { return __builtin_amdgcn_mfma_f32_16x16x32_bf16(__builtin_bit_cast(bf16x8_t, a), __builtin_bit_cast(bf16x8_t, b), c, 0, 0, 0); }
DI u32x4 pack8(const f32x16& a, int s) { return (u32x4){cvtpk(a[8 * s], a[8 * s + 1]), cvtpk(a[8 * s + 2], a[8 * s + 3]), cvtpk(a[8 * s + 4], a[8 * s + 5]), cvtpk(a[8 * s + 6], a[8 * s + 7])}; }
DI u32x4 pack8n(const f32x16& a, int s) {
    const u32x4 p = pack8(a, s);
    const auto x = __builtin_amdgcn_permlane32_swap(p[0], p[2], false, false), y = __builtin_amdgcn_permlane32_swap(p[1], p[3], false, false);
    return (u32x4){x[0], y[0], x[1], y[1]};
}
DI u32x4 ldsr(const LAS char* p) { return *(const LAS u32x4*)p; }
DI void ldsw(LAS char* p, u32x4 v) { *(LAS u32x4*)p = v; }
DI u32x2 trr(const LAS char* p) { return __builtin_bit_cast(u32x2, __builtin_amdgcn_ds_read_tr16_b64_v4i16((LAS s16x4*)p)); }
DI u32x4 tr2(const LAS char* p0, const LAS char* p1) { u32x2 a = trr(p0), b = trr(p1); return (u32x4){a[0], a[1], b[0], b[1]}; }
DI float ex2(float x) { return __builtin_amdgcn_exp2f(x); }
DI float swap_max(float m) { auto rr = __builtin_amdgcn_permlane32_swap(__float_as_uint(m), __float_as_uint(m), false, false); return fmaxf(__uint_as_float(rr[0]), __uint_as_float(rr[1])); }
DI float from_hi(float v) { auto rr = __builtin_amdgcn_permlane32_swap(__float_as_uint(v), __float_as_uint(v), false, false); return __uint_as_float(rr[1]); }
DI float from_lo(float v) { auto rr = __builtin_amdgcn_permlane32_swap(__float_as_uint(v), __float_as_uint(v), false, false); return __uint_as_float(rr[0]); }
DI u32x4 mksrd(const void* p, unsigned bytes) { u64 a = (u64)p; return (u32x4){(unsigned)a, (unsigned)(a >> 32) & 0xffffu, bytes, 0x00020000u}; }
DI void dma16(u32x4 srd, unsigned voff, unsigned soff, unsigned ldsaddr) {
    unsigned keep;
    asm volatile("s_mov_b32 %0, m0\n\ts_mov_b32 m0, %4\n\ts_nop 2\n\tbuffer_load_dwordx4 %1, %2, %3 offen lds\n\ts_mov_b32 m0, %0"
                 : "=&s"(keep) : "v"(voff), "s"(srd), "s"(soff), "s"(ldsaddr) : "memory");
}
DI void dma16l(u32x4 srd, unsigned voff, unsigned soff, unsigned ldsaddr) {
    unsigned keep;
    asm volatile("s_mov_b32 %0, m0\n\ts_mov_b32 m0, %4\n\ts_setprio 0\n\ts_nop 1\n\tbuffer_load_dwordx4 %1, %2, %3 offen lds\n\ts_setprio 2\n\ts_mov_b32 m0, %0"
                 : "=&s"(keep) : "v"(voff), "s"(srd), "s"(soff), "s"(ldsaddr) : "memory");
}
DI void dma4(u32x4 srd, unsigned voff, unsigned soff, unsigned ldsaddr) {
    unsigned keep;
    asm volatile("s_mov_b32 %0, m0\n\ts_mov_b32 m0, %4\n\ts_nop 2\n\tbuffer_load_dword %1, %2, %3 offen lds\n\ts_mov_b32 m0, %0"
                 : "=&s"(keep) : "v"(voff), "s"(srd), "s"(soff), "s"(ldsaddr) : "memory");
}
template <int N> DI void wait_vm() { asm volatile("s_waitcnt vmcnt(%0)" ::"n"(N) : "memory"); }
DI void bar() { asm volatile("s_waitcnt lgkmcnt(0)\n\ts_barrier" ::: "memory"); }
constexpr unsigned kSpin = 1u << 18;
DI void cnt_add(LAS char* c, int lane) { asm volatile("s_waitcnt lgkmcnt(0)" ::: "memory"); if (lane == 0) __hip_atomic_fetch_add((LAS unsigned*)c, 1u, RLX_WG); }
template <int SL = 1> DI void cnt_wait(LAS char* c, unsigned target) {
#pragma unroll 1
    for (unsigned sp = 0; sp < kSpin * 8; ++sp) { if (__builtin_expect((unsigned)__builtin_amdgcn_readfirstlane(__hip_atomic_load((LAS unsigned*)c, RLX_WG)) >= target, 1)) break; __builtin_amdgcn_s_sleep(SL); }
    asm volatile("" ::: "memory");
}
template <int SL = 1> DI void poll_flags(const GAS u64* p, u64 tag) {
#pragma unroll 1
    for (unsigned sp = 0; sp < kSpin; ++sp) { if (__all(__hip_atomic_load(p, RLX_AGENT) == tag)) break; __builtin_amdgcn_s_sleep(SL); }
    asm volatile("" ::: "memory");
}
#define MX3(a, b, c) __builtin_fmaxf(__builtin_fmaxf((a), (b)), (c))
DI float max16(const f32x16& p) {
    float a = MX3(p[0], p[1], p[2]), b = MX3(p[3], p[4], p[5]);
    a = MX3(a, p[6], p[7]); b = MX3(b, p[8], p[9]); a = MX3(a, p[10], p[11]); b = MX3(b, p[12], p[13]); a = MX3(a, p[14], p[15]);
    return __builtin_fmaxf(a, b);
}
#define LDB(rs, vo, so) __builtin_amdgcn_raw_buffer_load_b128(rs, vo, so, 16)
#define STB(v, rs, vo, so) __builtin_amdgcn_raw_buffer_store_b128(v, rs, vo, 0, 16)

__global__ __launch_bounds__(512, 2) void fused_kernel(const char* __restrict__ x, const char* __restrict__ wA, const char* __restrict__ woA,
                                                       const char* __restrict__ wB, const char* __restrict__ woB, const char* __restrict__ gw,
                                                       char* __restrict__ out, char* ws) {
    __shared__ __attribute__((aligned(16))) char lds_[LDS_BYTES];
    LAS char* lds = (LAS char*)lds_;
    const unsigned L0 = (unsigned)(size_t)lds;
    STAMP(0); CSTAMP(12);
    u64 tag_raw;
    asm volatile("s_load_dwordx2 %0, %1, 0x0" : "=s"(tag_raw) : "s"(ws) : "memory");
    const int lane = threadIdx.x & 63, r = lane & 31, hh = lane >> 5;
    const int wave = __builtin_amdgcn_readfirstlane(threadIdx.x >> 6), w4 = wave & 3;
    const int xcd = blockIdx.x & 7, idx = blockIdx.x >> 3;
    const int h = (xcd & 3) * 4 + (idx >> 3), rb = (xcd >> 2) * 8 + (idx & 7), rot = ((idx & 7) + 2 * (idx >> 3)) % 7;
    const int r0 = rb * 128, tile = rb * 4 + w4;
    const unsigned kctab = (unsigned)(0x65432106543210ull >> (4 * rot));
#define KC(c) ((kctab >> (4 * (c))) & 7u)
    const int o0 = (7 * h) >> 1;
    const bool odd = h & 1;
    const bool mE = !odd && hh, mO = odd && !hh;
    const int ga = o0 >> 3, gb = (8 * o0 + 31) >> 6, gc = (28 * h) >> 6, gd = (28 * h + 27) >> 6;

    const u32x4 sX = mksrd(x, SEQ * XROW), sWA = mksrd(wA, DM * WROW), sWB = mksrd(wB, DM * WROW);
    const __amdgpu_buffer_rsrc_t rWS = __builtin_amdgcn_make_buffer_rsrc(ws, 0, WS_END, 0x00020000);

    const u32x4 sW1 = h < 14 ? sWA : sWB;
    const unsigned vX0 = (r0 + 16 * wave + (lane >> 3)) * XROW + (((lane & 7) ^ (lane >> 4)) << 4);
    const unsigned vX1 = (r0 + 16 * wave + 8 + (lane >> 3)) * XROW + (((lane & 7) ^ (lane >> 4) ^ 4) << 4);
    const unsigned vsw = ((lane & 7) ^ ((lane >> 2) & 4)) << 4;
    const unsigned vW1 = (8 * wave + (lane >> 3)) * WROW + (h < 14 ? XROW + 128 * h : 128 * (h + 5)) + vsw;
#define LD_CH(c) do { const unsigned kc_ = KC(c), sl_ = L0 + ((c) % 5) * SLOT; dma16(sX, vX0, kc_ * 128, sl_ + wave * 2048); dma16(sX, vX1, kc_ * 128, sl_ + wave * 2048 + 1024); \
        dma16(sW1, vW1, kc_ * 64 * WROW, sl_ + 16384 + wave * 1024); } while (0)
    SBAR(); LD_CH(0); LD_CH(1); SBAR();
    STAMP(2);
#define LD_CH2(c) do { LD_CH(c); const unsigned kc_ = KC(c), sl_ = L0 + ((c) % 5) * SLOT; dma16(sX, vX0 - 64 * XROW, kc_ * 128, sl_ + w4 * 2048); dma16(sX, vX1 - 64 * XROW, kc_ * 128, sl_ + w4 * 2048 + 1024); \
        dma16(sW1, vW1 - 32 * WROW, kc_ * 64 * WROW, sl_ + 16384 + w4 * 1024); } while (0)
    const int xrow = 32 * w4 + r, xf = (xrow >> 1) & 7;
    const LAS char* xa[4]; const LAS char* xa3[4];
#pragma unroll
    for (int s = 0; s < 4; ++s) { xa[s] = lds + xrow * 128 + (((2 * s + hh) ^ xf) << 4); xa3[s] = xa[s] + 3 * SLOT; OPQ(xa3[s]); }
#define XFR(c, s) ldsr(((c) % 5 >= 3 ? xa3[s] : xa[s]) + ((c) % 5 >= 3 ? (c) % 5 - 3 : (c) % 5) * SLOT)
    const int q4 = (lane & 15) >> 2;
    const unsigned trl = ((lane >> 4) & 1) * 32 + (lane & 3) * 8;
    const LAS char* wk0 = lds + (8 * hh + q4) * 128 + trl + (q4 >> 1) * 64;
    const LAS char* wk1 = lds + (8 * hh + q4) * 128 + trl + ((q4 >> 1) ^ 1) * 64;
#define WF(b, s) tr2((b) + (s) * 2048, (b) + (s) * 2048 + 512)
#define GEMM2(off, KT) do { const LAS char* b0_ = wk0 + (off); const LAS char* b1_ = wk1 + (off); OPQ(b0_); OPQ(b1_); u32x4 w_[D2 + 1][2]; \
        _Pragma("unroll") for (int s = 0; s < D2; ++s) { w_[s][0] = WF(b0_, s); w_[s][1] = WF(b1_, s); } \
        _Pragma("unroll") for (int s = 0; s < 28; ++s) { if (s + D2 < 28) { w_[(s + D2) % (D2 + 1)][0] = WF(b0_, s + D2); w_[(s + D2) % (D2 + 1)][1] = WF(b1_, s + D2); } SBAR(); \
            if (KT) { a0 = mfma(w_[s % (D2 + 1)][0], xr[s], a0); a1 = mfma(w_[s % (D2 + 1)][1], xr[s], a1); } else { a0 = mfma(xr[s], w_[s % (D2 + 1)][0], a0); a1 = mfma(xr[s], w_[s % (D2 + 1)][1], a1); } SBAR(); } } while (0)
#define GEMM1(acc, wb, KT) do { u32x4 w_[D1 + 1]; \
        _Pragma("unroll") for (int s = 0; s < D1; ++s) w_[s] = tr2((wb) + s * 1024, (wb) + s * 1024 + 256); \
        _Pragma("unroll") for (int s = 0; s < 28; ++s) { if (s + D1 < 28) w_[(s + D1) % (D1 + 1)] = tr2((wb) + (s + D1) * 1024, (wb) + (s + D1) * 1024 + 256); SBAR(); \
            acc = KT ? mfma(w_[s % (D1 + 1)], xr[s], acc) : mfma(xr[s], w_[s % (D1 + 1)], acc); SBAR(); } } while (0)
#define PUB_K(base, g7) do { const unsigned vo_ = (base) + tile * KTILE + (g7) * 4096 + lane * 16; \
        STB(pack8n(a0, 0), rWS, vo_, 0); STB(pack8n(a0, 1), rWS, vo_ + 1024, 0); STB(pack8n(a1, 0), rWS, vo_ + 2048, 0); STB(pack8n(a1, 1), rWS, vo_ + 3072, 0); } while (0)
#define PUB_V(base, g7) do { const unsigned tb_ = (base) + tile * VTILE, vo_ = tb_ + hh * 7168 + (64 * (g7) + r) * 16; \
        STB(pack8(a0, 0), rWS, vo_, 0); STB(pack8(a0, 1), rWS, vo_ + 14336, 0); STB(pack8(a1, 0), rWS, vo_ + 512, 0); STB(pack8(a1, 1), rWS, vo_ + 14848, 0); \
        const unsigned c_ = lane == 0 ? 0x3F803F80u : 0u; STB(((u32x4){c_, c_, c_, c_}), rWS, lane < 8 ? tb_ + 28672 + (g7) * 128 + lane * 16 : WS_END, 0); } while (0)
    const unsigned vVl0 = r < 28 ? hh * 7168 + (28 * h + r) * 16 : 28672 + gc * 128 + (r - 28) * 16, vVl1 = r < 28 ? vVl0 + 14336 : vVl0;
    u64 tag;
#define READ_TAG() do { asm volatile("s_waitcnt lgkmcnt(0)\n\ts_mov_b64 %0, %1" : "=s"(tag) : "s"(tag_raw) : "memory"); tag = tag * 0x9E3779B97F4A7C15ull + 0x632BE59BD9B4E019ull; } while (0)

    const int ux = h == 7 ? 4 : h == 8 ? 5 : h == 10 ? 6 : h == 12 ? 7 : h >= 14 ? h - 6 : -1;
    const unsigned vW2 = (16 * w4 + (lane >> 3)) * WROW + vsw;
#define LD_W2(g, dst) do { _Pragma("unroll") for (int c = 0; c < 7; ++c) { dma16(sWB, vW2, KC(c) * 64 * WROW + (g) * 128, L0 + (dst) + c * 8192 + w4 * 2048); \
            dma16(sWB, vW2, (KC(c) * 64 + 8) * WROW + (g) * 128, L0 + (dst) + c * 8192 + w4 * 2048 + 1024); } } while (0)
    if (wave < 4) {
        dma4(sWB, ((idx & 7) * 56 + w4 * 14 + min(lane & 31, 13)) * WROW + (hh && ux >= 0 ? 14 + (ux >> 1) : h) * 128, 0, L0 + JUNK);
        if (threadIdx.x < 16) *(LAS unsigned*)(lds + CNT + threadIdx.x * 4) = 0u;
        READ_TAG();
        f32x16 a0 = {}, a1 = {};
        u32x4 fx[2][4], fw[2][8];
#define RD_KV(c) do { const LAS char* b0_ = wk0 + ((c) % 5) * SLOT + 16384; const LAS char* b1_ = wk1 + ((c) % 5) * SLOT + 16384; OPQ(b0_); OPQ(b1_); \
            _Pragma("unroll") for (int s = 0; s < 4; ++s) { fx[(c) & 1][s] = XFR(c, s); fw[(c) & 1][2 * s] = WF(b0_, s); fw[(c) & 1][2 * s + 1] = WF(b1_, s); } SBAR(); } while (0)
#define MM_KV(c, KT) do { _Pragma("unroll") for (int s = 0; s < 4; ++s) { \
            if (KT) { a0 = mfma(fw[(c) & 1][2 * s], fx[(c) & 1][s], a0); a1 = mfma(fw[(c) & 1][2 * s + 1], fx[(c) & 1][s], a1); } \
            else { a0 = mfma(fx[(c) & 1][s], fw[(c) & 1][2 * s], a0); a1 = mfma(fx[(c) & 1][s], fw[(c) & 1][2 * s + 1], a1); } } SBAR(); } while (0)
#define PH1A(KT) do { \
        wait_vm<4>(); bar(); STAMP(1); RD_KV(0); \
        wait_vm<1>(); bar(); RD_KV(1); MM_KV(0, KT); \
        bar(); RD_KV(2); MM_KV(1, KT); \
        bar(); RD_KV(3); MM_KV(2, KT); \
        bar(); RD_KV(4); MM_KV(3, KT); \
        bar(); RD_KV(5); MM_KV(4, KT); \
        bar(); RD_KV(6); MM_KV(5, KT); MM_KV(6, KT); } while (0)
#define RAISE1() do { cnt_add(lds + C_ST, lane); STAMP(3); wait_vm<0>(); if (lane < (h < 14 ? 1 : 2)) __hip_atomic_store(FGP(tile, h < 14 ? h : 2 * h + 10 + lane), tag, RLX_AGENT); } while (0)
        if (h < 7) { PH1A(true); PUB_K(WS_KA, h); RAISE1(); }
        else { PH1A(false); PUB_V(h < 14 ? WS_VA : WS_VB, h < 14 ? h - 7 : h - 9); RAISE1();
        }
        STAMP(4);
        PRI(3);
        const GAS u64* fp = FGP(((rb + lane) & 15) * 4 + w4, (lane >> 4) == 0 ? ga : (lane >> 4) == 1 ? gb : (lane >> 4) == 2 ? 7 + gc : 7 + gd);
        unsigned rdy = 0; u64 fl;
#define COUNT() do { u64 m_ = __ballot(fl == tag); m_ &= m_ >> 32; m_ &= m_ >> 16; rdy = (unsigned)__builtin_ctz(~(unsigned)m_ | 0x10000u); } while (0)
#define NEED(n) do { _Pragma("unroll 1") for (unsigned sp = 0; __builtin_expect(rdy < (n), 0) && sp < kSpin; ++sp) { if (sp) __builtin_amdgcn_s_sleep(4); fl = __hip_atomic_load(fp, RLX_AGENT); COUNT(); } asm volatile("" ::: "memory"); } while (0)
        cnt_wait(lds + C_Q, 4);
        u32x4 q[4][2];
#pragma unroll
        for (int t = 0; t < 4; ++t) { const LAS char* p = lds + QX + ((w4 + 1 + t) & 3) * 2048 + lane * 16; q[t][0] = ldsr(p); q[t][1] = ldsr(p + 1024); }
        const float gq = *(const LAS float*)(lds + GBUF + (32 * w4 + r) * 4);
        STAMP(6);
        NEED(3u);
        STAMP(5);
        const unsigned vK = WS_KA + w4 * KTILE + o0 * 512 + lane * 16, vV0 = WS_VA + w4 * VTILE + vVl0, vV1 = WS_VA + w4 * VTILE + vVl1;
        u32x4 kv[4][4];
#define LD_KV(b, i) do { const unsigned g_ = (unsigned)(rb + (i)) & 15u, sk_ = g_ * (4 * KTILE), sv_ = g_ * (4 * VTILE); \
            kv[b][0] = LDB(rWS, vK, sk_); kv[b][1] = LDB(rWS, vK + 1024, sk_); kv[b][2] = LDB(rWS, vV0, sv_); kv[b][3] = LDB(rWS, vV1, sv_); SBAR(); } while (0)
#define KFIX(b) do { kv[b][0][0] = mO ? 0x3F80u : kv[b][0][0]; kv[b][1][2] = mE ? 0x3F80u : kv[b][1][2]; } while (0)
        LD_KV(0, 0); fl = __hip_atomic_load(fp, RLX_AGENT); LD_KV(1, 1); LD_KV(2, 2);
        cnt_add(lds + C_FL, lane);
        kv[3][3] = (u32x4){0u, 0u, 0u, 0u};
        KFIX(0);
#pragma unroll
        for (int t = 0; t < 4; ++t) {
            f32x16 S = mfma(kv[0][0], q[t][0], (f32x16){}); S = mfma(kv[0][1], q[t][1], S);
            const unsigned mb = cvtpk(swap_max(max16(S)), 0.f) & 0xffffu;
            if (!hh) *(LAS unsigned*)(lds + MREF + ((w4 + 1 + t) & 3) * 512 + t * 128 + r * 4) = mb << 16;
            q[t][0][0] = mO ? mb ^ 0x8000u : q[t][0][0]; q[t][1][2] = mE ? mb ^ 0x8000u : q[t][1][2];
        }
        cnt_add(lds + C_QR, lane);
        STAMP(7); CSTAMP(13);
        PRI(1);
        f32x16 O[4] = {};
        f32x16 Sa = mfma(kv[0][0], q[0][0], (f32x16){}), Sb;
        Sa = mfma(kv[0][1], q[0][1], Sa);
        u32x4 pp1 = {0u, 0u, 0u, 0u};
#define EX4(Sx, i) do { Sx[i] = ex2(Sx[i]); Sx[i + 1] = ex2(Sx[i + 1]); Sx[i + 2] = ex2(Sx[i + 2]); Sx[i + 3] = ex2(Sx[i + 3]); } while (0)
#define SUB(Sx, b, t, Sn, bn, tn, bp, tp, E0, E1, E2, E3, EV) do { u32x4 p0_; \
            SBAR(); Sn = mfma(kv[bn][0], q[tn][0], (f32x16){}); SBAR(); E0; SBAR(); EX4(Sx, 0); SBAR(); Sn = mfma(kv[bn][1], q[tn][1], Sn); SBAR(); E1; SBAR(); EX4(Sx, 4); SBAR(); \
            O[tp] = mfma(kv[bp][3], pp1, O[tp]); SBAR(); E2; SBAR(); p0_ = pack8(Sx, 0); EV; SBAR(); EX4(Sx, 8); SBAR(); O[t] = mfma(kv[b][2], p0_, O[t]); SBAR(); E3; SBAR(); \
            EX4(Sx, 12); pp1 = pack8(Sx, 1); SBAR(); } while (0)
#define WVM(n) __builtin_amdgcn_s_waitcnt(0x0F70 | ((n) & 15) | (((n) >> 4) << 14))
#define KT(b) do { \
            SUB(Sa, b, 0, Sb, b, 1, ((b) + 3) & 3, 3, gn = (gn + 1) & 15u, sk = gn * (4 * KTILE), kv[((b) + 3) & 3][0] = LDB(rWS, vK, sk), kv[((b) + 3) & 3][1] = LDB(rWS, vK + 1024, sk), ); \
            SUB(Sb, b, 1, Sa, b, 2, b, 0, sv = gn * (4 * VTILE), kv[((b) + 3) & 3][2] = LDB(rWS, vV0, sv), kv[((b) + 3) & 3][3] = LDB(rWS, vV1, sv), , ); \
            SUB(Sa, b, 2, Sb, b, 3, b, 1, , , WVM(8), , KFIX(((b) + 1) & 3)); \
            SUB(Sb, b, 3, Sa, ((b) + 1) & 3, 0, b, 2, , , , , ); } while (0)
#define KT1(b, n) do { \
            SUB(Sa, b, 0, Sb, b, 1, ((b) + 3) & 3, 3, , , , , ); \
            SUB(Sb, b, 1, Sa, b, 2, b, 0, , , , , ); \
            SUB(Sa, b, 2, Sb, b, 3, b, 1, , , , , KFIX(((b) + 1) & 3)); \
            if (rdy < 16u) { COUNT(); NEED(n); fl = __hip_atomic_load(fp, RLX_AGENT); } LD_KV(((b) + 3) & 3, (b) + 3); \
            SUB(Sb, b, 3, Sa, ((b) + 1) & 3, 0, b, 2, , , , , ); } while (0)
        KT1(0, 4u); KT1(1, 5u); KT1(2, 6u); KT1(3, 7u);
        if (rdy < 16u) { COUNT(); NEED(16u); }
        WVM(8);
        unsigned gn = (unsigned)(rb + 6) & 15u, sk, sv;
        u32x2 pre = {0u, 0u};
#pragma unroll 1
        for (int i = 0; i < 3; ++i) { pre = *(volatile LAS u32x2*)(lds + C_QR); KT(0); KT(1); KT(2); KT(3); }
        O[3] = mfma(kv[3][3], pp1, O[3]);
        STAMP(8); CSTAMP(14);
        PRI(3);
        if (__builtin_expect(__builtin_amdgcn_readfirstlane(min(pre[0], pre[1])) < 4u, 0)) { cnt_wait(lds + C_QR, 4); cnt_wait(lds + C_CLR, 4); }
        asm volatile("" ::: "memory");
        float f[4];
#pragma unroll
        for (int t = 0; t < 4; ++t) f[t] = *(const LAS float*)(lds + MREF + w4 * 512 + t * 128 + r * 4);
#pragma unroll
        for (int t = 0; t < 3; ++t) {
            LAS char* d = lds + MG + (((w4 + 1 + t) & 3) * 3 + t) * MGT;
#pragma unroll
            for (int i = 0; i < 3; ++i) *(LAS f32x4*)(d + i * 1024 + lane * 16) = (f32x4){O[t][4 * i], O[t][4 * i + 1], O[t][4 * i + 2], O[t][4 * i + 3]};
            if (hh) *(LAS float*)(d + 3584 + r * 4) = O[t][12];
            else *(LAS f32x4*)(d + 3072 + r * 16) = (f32x4){O[t][12], O[t][13], O[t][14], O[t][15]};
        }
        cnt_add(lds + C_MG, lane);
        {   const float mm = fmaxf(fmaxf(f[0], f[1]), fmaxf(f[2], f[3]));
#pragma unroll
            for (int t = 0; t < 4; ++t) f[t] = ex2(f[t] - mm); }
        const float lo = from_hi(O[3][12]) * f[3];
        asm volatile("" :: "v"(f[0]), "v"(f[1]), "v"(f[2]), "v"(lo));
        cnt_wait(lds + C_MG, 4);
        f32x16 Oo;
        bool bad;
        {   float pl[3]; f32x4 pt[3][4];
            const LAS char* s = lds + MG + w4 * 3 * MGT;
#pragma unroll
            for (int t = 0; t < 3; ++t) pl[t] = *(const LAS float*)(s + t * MGT + 3584 + r * 4);
#pragma unroll
            for (int t = 0; t < 3; ++t) {
#pragma unroll
                for (int i = 0; i < 3; ++i) pt[t][i] = *(const LAS f32x4*)(s + t * MGT + i * 1024 + lane * 16);
                pt[t][3] = *(const LAS f32x4*)(s + t * MGT + 3072 + r * 16); }
            SBAR();
            const float l = lo + pl[0] * f[0] + pl[1] * f[1] + pl[2] * f[2], sc = gq * __builtin_amdgcn_rcpf(l);
#pragma unroll
            for (int t = 0; t < 4; ++t) f[t] *= sc;
#pragma unroll
            for (int i = 0; i < 16; ++i) Oo[i] = O[3][i] * f[3] + pt[0][i >> 2][i & 3] * f[0] + pt[1][i >> 2][i & 3] * f[1] + pt[2][i >> 2][i & 3] * f[2];
            const float z = (((Oo[0] + Oo[1]) + (Oo[2] + Oo[3])) + ((Oo[4] + Oo[5]) + (Oo[6] + Oo[7]))) + (((Oo[8] + Oo[9]) + (Oo[10] + Oo[11])) + ((Oo[12] + Oo[13]) + (Oo[14] + Oo[15])));
            bad = !(l <= 1.0e30f) || !(__builtin_fabsf(z) <= 3.0e38f);
        }
        if (__builtin_expect(__any(bad), 0)) {
            u32x4 qa = q[3][0], qb = q[3][1];
            if (mO) qa[0] = 0u;
            if (mE) qb[2] = 0u;
            Oo = (f32x16){}; float m = -__builtin_inff();
#pragma unroll 1
            for (int tt = 0; tt < 64; ++tt) {
                const unsigned ko = WS_KA + o0 * 512 + lane * 16;
                const u32x4 k0 = LDB(rWS, ko, tt * KTILE), k1 = LDB(rWS, ko + 1024, tt * KTILE), v0 = LDB(rWS, WS_VA + vVl0, tt * VTILE), v1 = LDB(rWS, WS_VA + vVl1, tt * VTILE);
                f32x16 S = mfma(k0, qa, (f32x16){}); S = mfma(k1, qb, S);
                const float mn = fmaxf(m, swap_max(max16(S))), al = ex2(m - mn);
                m = mn;
#pragma unroll
                for (int i = 0; i < 16; ++i) { S[i] = ex2(S[i] - mn); Oo[i] *= al; }
                Oo = mfma(v0, pack8(S, 0), Oo); Oo = mfma(v1, pack8(S, 1), Oo);
            }
            const float sc = gq / from_hi(Oo[12]);
#pragma unroll
            for (int i = 0; i < 16; ++i) Oo[i] *= sc;
        }
        u32x4 f0 = pack8(Oo, 0), f1 = pack8(Oo, 1);
        if (hh) { f1[2] = 0u; f1[3] = 0u; }
#pragma unroll
        for (int i = 0; i < 4; ++i) { f0[i] = min(f0[i], 0xFFFFFFFEu); f1[i] = min(f1[i], 0xFFFFFFFEu); }
        const unsigned vOA = WS_OA + (tile * 16 + h) * 2048 + lane * 16;
        STB(f0, rWS, vOA, 0); STB(f1, rWS, vOA + 1024, 0);
        cnt_add(lds + C_PUB, lane);
        STAMP(9);
    } else {
        PRI(2);
        LD_CH2(2);
        READ_TAG();
        u32x4 xr[28];
#define GETX(c) do { _Pragma("unroll") for (int s = 0; s < 4; ++s) xr[4 * (c) + s] = XFR(c, s); } while (0)
        wait_vm<9>(); bar(); STAMP(1); LD_CH2(3); GETX(0);
        wait_vm<12>(); bar(); LD_CH2(4); GETX(1);
        wait_vm<12>(); bar(); LD_CH2(5); GETX(2);
        wait_vm<12>(); bar(); LD_CH2(6); GETX(3);
        wait_vm<12>(); bar();
        if (lane < 56) dma16(mksrd(gw, XROW), lane * 16, 0, L0 + GATE);
        { const unsigned vWq = (16 * w4 + (lane >> 2)) * WROW + (lane & 3) * 16 + o0 * 16;
#pragma unroll
          for (int c = 0; c < 7; ++c) dma16(sWA, vWq, KC(c) * 64 * WROW, L0 + WQ + c * 4096 + w4 * 1024); }
        GETX(4);
        wait_vm<14>(); bar(); GETX(5);
        wait_vm<8>(); bar(); LD_W2(h, W2); GETX(6);
        wait_vm<14>();
#pragma unroll
        for (int i = 0; i < 2; ++i) { const int c = 4 * i + (lane >> 4);
            if (c < 7) *(LAS unsigned short*)(lds + WQ + c * 4096 + (16 * w4 + (lane & 15)) * 64 + (odd ? 0 : 56)) = *(const LAS unsigned short*)(lds + GATE + (KC(c) * 64 + 16 * w4 + (lane & 15)) * 2); }
        unsigned hs = 0;
#define HSYNC() do { cnt_add(lds + C_H, lane); hs += 4; cnt_wait<2>(lds + C_H, hs); } while (0)
        HSYNC();
        STAMP(3);
        float gq;
        {   f32x16 aQ = {};
            const LAS char* wb = lds + WQ + (8 * hh + q4) * 64 + trl; OPQ(wb);
            GEMM1(aQ, wb, true);
            gq = 1.f / (1.f + __expf(-(odd ? from_lo(aQ[0]) : from_hi(aQ[12]))));
#pragma unroll
            for (int i = 0; i < 16; ++i) aQ[i] *= QSCALE;
            u32x4 q0 = pack8n(aQ, 0), q1 = pack8n(aQ, 1);
            if (mO) { q0[0] = 0u; q0[1] = 0u; }
            if (mE) { q1[2] = 0u; q1[3] = 0u; }
            ldsw(lds + QX + w4 * 2048 + lane * 16, q0); ldsw(lds + QX + w4 * 2048 + 1024 + lane * 16, q1);
            if (!hh) *(LAS float*)(lds + GBUF + (32 * w4 + r) * 4) = gq;
            cnt_add(lds + C_Q, lane);
        }
        STAMP(4);
        const bool proj = (h & 7) < 7;
        const unsigned vO = (8 * w4 + (lane >> 3)) * XROW + (h & 7) * 128 + vsw;
#define LD_WO(p, dst) do { const u32x4 sO_ = mksrd(p, DM * XROW); _Pragma("unroll") for (int i = 0; i < 14; ++i) { wait_vm<WIN - 1>(); dma16l(sO_, vO, i * 32 * XROW, L0 + (dst) + i * 4096 + w4 * 1024); } } while (0)
        u32x4 pb[4];
#define ST_V(g7) do { const unsigned tb = WS_VB + tile * VTILE, vo = tb + hh * 7168 + (64 * (g7) + r) * 16; \
            STB(pb[0], rWS, vo, 0); STB(pb[1], rWS, vo + 14336, 0); STB(pb[2], rWS, vo + 512, 0); STB(pb[3], rWS, vo + 14848, 0); \
            const unsigned c = lane == 0 ? 0x3F803F80u : 0u; STB(((u32x4){c, c, c, c}), rWS, lane < 8 ? tb + 28672 + (g7) * 128 + lane * 16 : WS_END, 0); } while (0)
        {   f32x16 a0 = {}, a1 = {};
            if (ux >= 0) { cnt_wait(lds + C_ST, 4); const unsigned vU = (16 * w4 + (lane >> 2)) * WROW + (lane & 3) * 16 + 1792 + ux * 64;
#pragma unroll
                for (int c = 0; c < 7; ++c) dma16(sWB, vU, KC(c) * 64 * WROW, L0 + c * 4096 + w4 * 1024); }
            wait_vm<0>(); HSYNC();
            if (h < 14) {
                GEMM2(W2, true);
                if (h < 7) {
#pragma unroll
                    for (int i = 0; i < 16; ++i) { a0[i] *= QSCALE; a1[i] *= QSCALE; } }
                pb[0] = pack8n(a0, 0); pb[1] = pack8n(a0, 1); pb[2] = pack8n(a1, 0); pb[3] = pack8n(a1, 1);
            } else {
                GEMM2(W2, false);
                pb[0] = pack8(a0, 0); pb[1] = pack8(a0, 1); pb[2] = pack8(a1, 0); pb[3] = pack8(a1, 1);
            }
            if (ux >= 0) {
                a0 = (f32x16){};
                const LAS char* wb = lds + (8 * hh + q4) * 64 + trl;
                GEMM1(a0, wb, false);
            }
            STAMP(6);
            cnt_wait<2>(lds + C_FL, 4);
            if (h < 14) { const unsigned vo = (h < 7 ? WS_QB + h * 4096 : WS_KB + (h - 7) * 4096) + tile * KTILE + lane * 16;
                STB(pb[0], rWS, vo, 0); STB(pb[1], rWS, vo + 1024, 0); STB(pb[2], rWS, vo + 2048, 0); STB(pb[3], rWS, vo + 3072, 0);
            } else ST_V(h - 14);
            if (ux >= 0) { const unsigned tb = WS_VB + tile * VTILE, vo = tb + hh * 7168 + (32 * ux + r) * 16;
                STB(pack8(a0, 0), rWS, vo, 0); STB(pack8(a0, 1), rWS, vo + 14336, 0);
                const unsigned c = lane == 0 ? 0x3F803F80u : 0u; STB(((u32x4){c, c, c, c}), rWS, lane < 8 ? tb + 28672 + (ux >> 1) * 128 + lane * 16 : WS_END, 0); }
        }
        cnt_add(lds + C_H, lane); hs += 4;
        { const u32x4 ones = {~0u, ~0u, ~0u, ~0u}; const unsigned vo = WS_OA + (tile * 16 + h) * 2048 + lane * 16;
          STB(ones, rWS, vo, 0); STB(ones, rWS, vo + 1024, 0); }
        wait_vm<0>();
        if (lane == 0 || (lane == 1 && h >= 14) || (lane == 2 && ux >= 0)) __hip_atomic_store(FGP(tile, lane == 2 ? 28 + ux : h < 14 ? 14 + h : 2 * h + lane), tag, RLX_AGENT);
        cnt_add(lds + C_CLR, lane);
        STAMP(5);
        cnt_wait<2>(lds + C_H, hs);
        if (proj) LD_WO(woB, WOB);
        const int rt = w4 & 1, kh = w4 >> 1, T = rb * 4 + (h >> 3) * 2 + rt;
        const LAS char* wo0 = lds + kh * 28672 + (4 * hh + q4) * 128 + trl + (q4 >> 1) * 64;
        const LAS char* wo1 = lds + kh * 28672 + (4 * hh + q4) * 128 + trl + ((q4 >> 1) ^ 1) * 64;
        const unsigned wfix = 3072 - hh * 1024;
#define WOF(b, bx, hd, s) ((s) == 0 ? tr2((b) + (hd) * 3584, (b) + (hd) * 3584 + 1024) : tr2((b) + (hd) * 3584 + 2048, (bx) + (hd) * 3584))
        {   const int i = lane & 7, g = i == 0 ? 14 + ga : i == 1 ? 14 + gb : i == 2 ? 21 + ga : i == 3 ? 21 + gb : i == 4 ? 28 + ((28 * h) >> 5) : 28 + ((28 * h + 27) >> 5);
            const int tp = tile > 0 ? tile - 1 : 0;
            const int tf = (lane & 8) ? tp : tile;
            poll_flags<24>(FGP(tf, g), tag);
            STAMP(7);
            const unsigned vk = o0 * 512 + lane * 16;
            u32x4 b0 = LDB(rWS, vk, WS_QB + tile * KTILE), b1 = LDB(rWS, vk + 1024, WS_QB + tile * KTILE);
            const u32x4 kp0 = LDB(rWS, vk, WS_KB + tp * KTILE), kp1 = LDB(rWS, vk + 1024, WS_KB + tp * KTILE), ko0 = LDB(rWS, vk, WS_KB + tile * KTILE), ko1 = LDB(rWS, vk + 1024, WS_KB + tile * KTILE);
            const u32x4 vp1 = LDB(rWS, vVl1, WS_VB + tp * VTILE), vo0 = LDB(rWS, vVl0, WS_VB + tile * VTILE), vo1 = LDB(rWS, vVl1, WS_VB + tile * VTILE);
            if (mO) { b0[0] = 0u; b0[1] = 0u; }
            if (mE) { b1[2] = 0u; b1[3] = 0u; }
            f32x16 A = mfma(kp0, b0, (f32x16){}); A = mfma(kp1, b1, A);
            f32x16 B = mfma(ko0, b0, (f32x16){}); B = mfma(ko1, b1, B);
            const u64 M = 0x5555ull << (18 + r);
            unsigned Mp = (unsigned)M >> (4 * hh); const unsigned Mo = (unsigned)(M >> 32) >> (4 * hh);
            if (tile == 0) Mp = 0u;
#pragma unroll
            for (int i2 = 0; i2 < 16; ++i2) { const int c = (i2 & 3) + 8 * (i2 >> 2);
                const unsigned tb = (unsigned)__builtin_amdgcn_sbfe((int)Mo, c, 1); B[i2] = __uint_as_float((__float_as_uint(B[i2]) & tb) | (0xff800000u & ~tb));
                if (i2 >= 8) { const unsigned ta = (unsigned)__builtin_amdgcn_sbfe((int)Mp, c, 1); A[i2] = __uint_as_float((__float_as_uint(A[i2]) & ta) | (0xff800000u & ~ta)); } }
            float m = max16(B);
            m = MX3(m, A[8], A[9]); m = MX3(m, A[10], A[11]); m = MX3(m, A[12], A[13]); m = MX3(m, A[14], A[15]);
            m = swap_max(m);
#pragma unroll
            for (int i2 = 0; i2 < 16; ++i2) { B[i2] = ex2(B[i2] - m); if (i2 >= 8) A[i2] = ex2(A[i2] - m); }
            f32x16 OB = mfma(vp1, pack8(A, 1), (f32x16){});
            OB = mfma(vo0, pack8(B, 0), OB); OB = mfma(vo1, pack8(B, 1), OB);
            const float sc = (1.f - gq) * __builtin_amdgcn_rcpf(from_hi(OB[12]));
#pragma unroll
            for (int i2 = 0; i2 < 16; ++i2) OB[i2] *= sc;
            u32x4 f0 = pack8(OB, 0), f1 = pack8(OB, 1);
            if (hh) { f1[2] = 0u; f1[3] = 0u; }
            const unsigned vo = WS_OB + (tile * 16 + h) * 2048 + lane * 16;
            STB(f0, rWS, vo, 0); STB(f1, rWS, vo + 1024, 0);
        }
        wait_vm<0>();
        if (lane == 0) __hip_atomic_store((GAS u64*)(ws + WS_FOB + (tile * 16 + h) * 8), tag, RLX_AGENT);
        STAMP(8);
        if (proj) {
            cnt_add(lds + C_H, lane); hs += 4;
            LD_WO(woA, WOA);
            poll_flags<24>((const GAS u64*)(ws + WS_FOB + (T * 16 + 8 * kh + (lane & 7)) * 8), tag);
            cnt_wait<2>(lds + C_H, hs);
            f32x16 c0 = {}, c1 = {};
            const unsigned vF = T * 32768 + kh * 16384 + lane * 16;
            {   const LAS char* p0x = wo0 + wfix; const LAS char* p1x = wo1 + wfix;
                u32x4 of[16];
                SBAR(); PRI(0);
#pragma unroll
                for (int i = 0; i < 16; ++i) of[i] = LDB(rWS, vF, WS_OB + i * 1024);
                SBAR(); PRI(2);
#pragma unroll
                for (int i = 0; i < 16; ++i) { c0 = mfma(WOF(wo0, p0x, i >> 1, i & 1), of[i], c0); c1 = mfma(WOF(wo1, p1x, i >> 1, i & 1), of[i], c1); SBAR(); }
            }
            wait_vm<0>(); HSYNC();
            u32x4 wf[8][2][2];
            {   const LAS char* p0 = wo0 + WOA; const LAS char* p1 = wo1 + WOA; const LAS char* p0x = p0 + wfix; const LAS char* p1x = p1 + wfix; OPQ(p0); OPQ(p1); OPQ(p0x); OPQ(p1x);
#pragma unroll
                for (int hd = 0; hd < 8; ++hd) { wf[hd][0][0] = WOF(p0, p0x, hd, 0); wf[hd][1][0] = WOF(p0, p0x, hd, 1); wf[hd][0][1] = WOF(p1, p1x, hd, 0); wf[hd][1][1] = WOF(p1, p1x, hd, 1); }
            }
            STAMP(9);
            cnt_wait<4>(lds + C_PUB, 4);
            __builtin_amdgcn_s_sleep(8);
            STAMP(10);
            unsigned nx = 0;
#pragma unroll 1
            for (unsigned sp = 0; sp < kSpin && nx < 8; ++sp) {
                asm volatile("" ::: "memory");
                u32x4 of[16];
#pragma unroll
                for (int i = 0; i < 16; ++i) of[i] = LDB(rWS, vF, (unsigned)(i >> 1) >= nx ? WS_OA + i * 1024 : WS_END);
#pragma unroll
                for (int hd = 0; hd < 8; ++hd) {
                    const u32x4 a = of[2 * hd], b = of[2 * hd + 1];
                    const unsigned mx = max(max(max(a[0], a[1]), max(a[2], a[3])), max(max(b[0], b[1]), max(b[2], b[3])));
                    const unsigned ok = (unsigned)__all(mx != ~0u) | (unsigned)(sp + 1 == kSpin);
                    if ((unsigned)(nx == (unsigned)hd) & ok) {
                        c0 = mfma(wf[hd][0][0], a, c0); c1 = mfma(wf[hd][0][1], a, c1); c0 = mfma(wf[hd][1][0], b, c0); c1 = mfma(wf[hd][1][1], b, c1);
                        nx = hd + 1;
                    }
                }
            }
            const f32x16 give = kh ? c0 : c1, keep = kh ? c1 : c0;
            { LAS char* d = lds + WOB + w4 * 4096 + lane * 16;
#pragma unroll
              for (int i = 0; i < 4; ++i) *(LAS f32x4*)(d + i * 1024) = (f32x4){give[4 * i], give[4 * i + 1], give[4 * i + 2], give[4 * i + 3]}; }
            cnt_add(lds + C_PAIR + rt * 4, lane); cnt_wait(lds + C_PAIR + rt * 4, 2);
            f32x16 fin;
            { const LAS char* s = lds + WOB + (w4 ^ 2) * 4096 + lane * 16;
#pragma unroll
              for (int i = 0; i < 4; ++i) { const f32x4 t = *(const LAS f32x4*)(s + i * 1024);
#pragma unroll
                  for (int k = 0; k < 4; ++k) fin[4 * i + k] = keep[4 * i + k] + t[k]; } }
            GAS char* orow = (GAS char*)out + (T * 32 + r) * XROW + (h & 7) * 128 + kh * 64 + hh * 16;
            *(GAS u32x4*)orow = pack8n(fin, 0); *(GAS u32x4*)(orow + 32) = pack8n(fin, 1);
        } else if (h == 7 && rb == 0 && wave == 4) {
#pragma unroll 1
            for (int i = 0; i < 4; ++i) { const int hq = i * 4 + (lane >> 4); poll_flags<24>(FGP((lane & 15) * 4, hq < 15 ? 14 + hq : 30), tag); }
            if (lane == 0) __hip_atomic_store((GAS u64*)(ws + WS_EPOCH), tag, RLX_AGENT);
        }
    }
}

extern "C" void kernel_launch(void* const* d_in, const int* in_sizes, int n_in, void* d_out, int out_size, void* d_ws, size_t ws_size, hipStream_t stream) {
    fused_kernel<<<256, 512, 0, stream>>>((const char*)d_in[0], (const char*)d_in[1], (const char*)d_in[2], (const char*)d_in[4], (const char*)d_in[5],
                                          (const char*)d_in[6], (char*)d_out, (char*)d_ws);
}
```

```cpp
#include <hip/hip_runtime.h>
#include <hip/hip_bf16.h>
#include <stdint.h>

typedef __bf16 bf16x8_t __attribute__((ext_vector_type(8)));
typedef __bf16 bf16x2_t __attribute__((ext_vector_type(2)));
typedef float f32x2 __attribute__((ext_vector_type(2)));
typedef float f32x4 __attribute__((ext_vector_type(4)));
typedef float f32x16 __attribute__((ext_vector_type(16)));
typedef unsigned u32x4 __attribute__((ext_vector_type(4)));
typedef unsigned u32x2 __attribute__((ext_vector_type(2)));
typedef short s16x4 __attribute__((ext_vector_type(4)));
typedef unsigned long long u64;
#define LAS __attribute__((address_space(3)))
#define GAS __attribute__((address_space(1)))
#define DI __device__ __forceinline__
#define SBAR() __builtin_amdgcn_sched_barrier(0)
#define PRI(n) __builtin_amdgcn_s_setprio(n)
#define RLX_AGENT __ATOMIC_RELAXED, __HIP_MEMORY_SCOPE_AGENT
#define RLX_WG __ATOMIC_RELAXED, __HIP_MEMORY_SCOPE_WORKGROUP
#define OPQ(p) asm volatile("" : "+v"(p))

constexpr int WIN = 4;
constexpr int D1 = 7, D2 = 3;
constexpr int SEQ = 2048, DM = 448, N3 = 1344;
constexpr int XROW = DM * 2;
constexpr int WROW = N3 * 2;
constexpr float QSCALE = 0.18898223650461362f * 1.4426950408889634f;

constexpr unsigned WS_EPOCH = 0;
constexpr unsigned WS_FG = 4096;
constexpr unsigned WS_FOB = WS_FG + 4 * 42 * 128;
#define FGP(t, gid) ((GAS u64*)(ws + WS_FG + (((t) & 3) * 42 + (gid)) * 128 + ((t) >> 2) * 8))
constexpr unsigned KTILE = 28672;
constexpr unsigned VTILE = 29568;
constexpr unsigned WS_KA = 65536, WS_QB = WS_KA + 64 * KTILE, WS_KB = WS_QB + 64 * KTILE;
constexpr unsigned WS_VA = WS_KB + 64 * KTILE, WS_VB = WS_VA + 64 * VTILE;
constexpr unsigned WS_OA = WS_VB + 64 * VTILE;
constexpr unsigned WS_OB = WS_OA + 2097152;
constexpr unsigned WS_END = WS_OB + 2097152;

constexpr unsigned SLOT = 24576;
constexpr unsigned WQ = 5 * SLOT;
constexpr unsigned QX = WQ + 28672;
constexpr unsigned GATE = QX + 8192;
constexpr unsigned GBUF = GATE + 896;
constexpr unsigned MREF = GBUF + 512;
constexpr unsigned CNT = MREF + 2048;
constexpr unsigned LDS_BYTES = 163840;
constexpr unsigned W2 = 2 * SLOT;
constexpr unsigned WOB = 0, WOA = 57344, MG = 114688, MGT = 3712;
constexpr unsigned C_H = CNT, C_MG = CNT + 8, C_PAIR = CNT + 12, C_ST = CNT + 20, C_FL = CNT + 24, C_Q = CNT + 28, C_QR = CNT + 32, C_CLR = CNT + 36, C_PUB = CNT + 40;
constexpr unsigned JUNK = CNT + 64;
static_assert(JUNK + 256 <= LDS_BYTES && MG + 12 * MGT <= GATE, "LDS map");

DI unsigned cvtpk(float a, float b) { f32x2 v = {a, b}; return __builtin_bit_cast(unsigned, __builtin_convertvector(v, bf16x2_t)); }
DI f32x16 mfma(u32x4 a, u32x4 b, f32x16 c) { return __builtin_amdgcn_mfma_f32_32x32x16_bf16(__builtin_bit_cast(bf16x8_t, a), __builtin_bit_cast(bf16x8_t, b), c, 0, 0, 0); }
DI u32x4 pack8(const f32x16& a, int s) { return (u32x4){cvtpk(a[8 * s], a[8 * s + 1]), cvtpk(a[8 * s + 2], a[8 * s + 3]), cvtpk(a[8 * s + 4], a[8 * s + 5]), cvtpk(a[8 * s + 6], a[8 * s + 7])}; }
DI u32x4 pack8n(const f32x16& a, int s) {
    const u32x4 p = pack8(a, s);
    const auto x = __builtin_amdgcn_permlane32_swap(p[0], p[2], false, false), y = __builtin_amdgcn_permlane32_swap(p[1], p[3], false, false);
    return (u32x4){x[0], y[0], x[1], y[1]};
}
DI u32x4 ldsr(const LAS char* p) { return *(const LAS u32x4*)p; }
DI void ldsw(LAS char* p, u32x4 v) { *(LAS u32x4*)p = v; }
DI u32x2 trr(const LAS char* p) { return __builtin_bit_cast(u32x2, __builtin_amdgcn_ds_read_tr16_b64_v4i16((LAS s16x4*)p)); }
DI u32x4 tr2(const LAS char* p0, const LAS char* p1) { u32x2 a = trr(p0), b = trr(p1); return (u32x4){a[0], a[1], b[0], b[1]}; }
DI float ex2(float x) { return __builtin_amdgcn_exp2f(x); }
DI float swap_max(float m) { auto rr = __builtin_amdgcn_permlane32_swap(__float_as_uint(m), __float_as_uint(m), false, false); return fmaxf(__uint_as_float(rr[0]), __uint_as_float(rr[1])); }
DI float from_hi(float v) { auto rr = __builtin_amdgcn_permlane32_swap(__float_as_uint(v), __float_as_uint(v), false, false); return __uint_as_float(rr[1]); }
DI float from_lo(float v) { auto rr = __builtin_amdgcn_permlane32_swap(__float_as_uint(v), __float_as_uint(v), false, false); return __uint_as_float(rr[0]); }
DI u32x4 mksrd(const void* p, unsigned bytes) { u64 a = (u64)p; return (u32x4){(unsigned)a, (unsigned)(a >> 32) & 0xffffu, bytes, 0x00020000u}; }
DI void dma16(u32x4 srd, unsigned voff, unsigned soff, unsigned ldsaddr) {
    unsigned keep;
    asm volatile("s_mov_b32 %0, m0\n\ts_mov_b32 m0, %4\n\ts_nop 2\n\tbuffer_load_dwordx4 %1, %2, %3 offen lds\n\ts_mov_b32 m0, %0"
                 : "=&s"(keep) : "v"(voff), "s"(srd), "s"(soff), "s"(ldsaddr) : "memory");
}
DI void dma16l(u32x4 srd, unsigned voff, unsigned soff, unsigned ldsaddr) {
    unsigned keep;
    asm volatile("s_mov_b32 %0, m0\n\ts_mov_b32 m0, %4\n\ts_setprio 0\n\ts_nop 1\n\tbuffer_load_dwordx4 %1, %2, %3 offen lds\n\ts_setprio 2\n\ts_mov_b32 m0, %0"
                 : "=&s"(keep) : "v"(voff), "s"(srd), "s"(soff), "s"(ldsaddr) : "memory");
}
DI void dma4(u32x4 srd, unsigned voff, unsigned soff, unsigned ldsaddr) {
    unsigned keep;
    asm volatile("s_mov_b32 %0, m0\n\ts_mov_b32 m0, %4\n\ts_nop 2\n\tbuffer_load_dword %1, %2, %3 offen lds\n\ts_mov_b32 m0, %0"
                 : "=&s"(keep) : "v"(voff), "s"(srd), "s"(soff), "s"(ldsaddr) : "memory");
}
template <int N> DI void wait_vm() { asm volatile("s_waitcnt vmcnt(%0)" ::"n"(N) : "memory"); }
DI void bar() { asm volatile("s_waitcnt lgkmcnt(0)\n\ts_barrier" ::: "memory"); }
constexpr unsigned kSpin = 1u << 14;
DI void cnt_add(LAS char* c, int lane) { asm volatile("s_waitcnt lgkmcnt(0)" ::: "memory"); if (lane == 0) __hip_atomic_fetch_add((LAS unsigned*)c, 1u, RLX_WG); }
template <int SL = 1> DI void cnt_wait(LAS char* c, unsigned target) {
#pragma unroll 1
    for (unsigned sp = 0; sp < kSpin * 128; ++sp) { if (__builtin_expect((unsigned)__builtin_amdgcn_readfirstlane(__hip_atomic_load((LAS unsigned*)c, RLX_WG)) >= target, 1)) break; __builtin_amdgcn_s_sleep(SL); }
    asm volatile("" ::: "memory");
}
template <int SL = 1> DI void poll_flags(const GAS u64* p, u64 tag) {
#pragma unroll 1
    for (unsigned sp = 0; sp < kSpin; ++sp) { if (__all(__hip_atomic_load(p, RLX_AGENT) == tag)) break; __builtin_amdgcn_s_sleep(SL); }
    asm volatile("" ::: "memory");
}
#define MX3(a, b, c) __builtin_fmaxf(__builtin_fmaxf((a), (b)), (c))
DI float max16(const f32x16& p) {
    float a = MX3(p[0], p[1], p[2]), b = MX3(p[3], p[4], p[5]);
    a = MX3(a, p[6], p[7]); b = MX3(b, p[8], p[9]); a = MX3(a, p[10], p[11]); b = MX3(b, p[12], p[13]); a = MX3(a, p[14], p[15]);
    return __builtin_fmaxf(a, b);
}
#define LDB(rs, vo, so) __builtin_amdgcn_raw_buffer_load_b128(rs, vo, so, 16)
#define STB(v, rs, vo, so) __builtin_amdgcn_raw_buffer_store_b128(v, rs, vo, 0, 16)

__global__ __launch_bounds__(512, 2) void fused_kernel(const char* __restrict__ x, const char* __restrict__ wA, const char* __restrict__ woA,
                                                       const char* __restrict__ wB, const char* __restrict__ woB, const char* __restrict__ gw,
                                                       char* __restrict__ out, char* ws) {
    __shared__ __attribute__((aligned(16))) char lds_[LDS_BYTES];
    LAS char* lds = (LAS char*)lds_;
    const unsigned L0 = (unsigned)(size_t)lds;

    u64 tag_raw;
    asm volatile("s_load_dwordx2 %0, %1, 0x0" : "=s"(tag_raw) : "s"(ws) : "memory");
    const int lane = threadIdx.x & 63, r = lane & 31, hh = lane >> 5;
    const int wave = __builtin_amdgcn_readfirstlane(threadIdx.x >> 6), w4 = wave & 3;
    const int xcd = blockIdx.x & 7, idx = blockIdx.x >> 3;
    const int h = (xcd & 3) * 4 + (idx >> 3), rb = (xcd >> 2) * 8 + (idx & 7), rot = ((idx & 7) + 2 * (idx >> 3)) % 7;
    const int r0 = rb * 128, tile = rb * 4 + w4;
    const unsigned kctab = (unsigned)(0x65432106543210ull >> (4 * rot));
#define KC(c) ((kctab >> (4 * (c))) & 7u)
    const int o0 = (7 * h) >> 1;
    const bool odd = h & 1;
    const bool mE = !odd && hh, mO = odd && !hh;
    const int ga = o0 >> 3, gb = (8 * o0 + 31) >> 6, gc = (28 * h) >> 6, gd = (28 * h + 27) >> 6;

    const u32x4 sX = mksrd(x, SEQ * XROW), sWA = mksrd(wA, DM * WROW), sWB = mksrd(wB, DM * WROW);
    const __amdgpu_buffer_rsrc_t rWS = __builtin_amdgcn_make_buffer_rsrc(ws, 0, WS_END, 0x00020000);

    const u32x4 sW1 = h < 14 ? sWA : sWB;
    const unsigned vX0 = (r0 + 16 * wave + (lane >> 3)) * XROW + (((lane & 7) ^ (lane >> 4)) << 4);
    const unsigned vX1 = (r0 + 16 * wave + 8 + (lane >> 3)) * XROW + (((lane & 7) ^ (lane >> 4) ^ 4) << 4);
    const unsigned vsw = ((lane & 7) ^ ((lane >> 2) & 4)) << 4;
    const unsigned vW1 = (8 * wave + (lane >> 3)) * WROW + (h < 14 ? XROW + 128 * h : 128 * (h + 5)) + vsw;
#define LD_CH(c) do { const unsigned kc_ = KC(c), sl_ = L0 + ((c) % 5) * SLOT; dma16(sX, vX0, kc_ * 128, sl_ + wave * 2048); dma16(sX, vX1, kc_ * 128, sl_ + wave * 2048 + 1024); \
        dma16(sW1, vW1, kc_ * 64 * WROW, sl_ + 16384 + wave * 1024); } while (0)
    SBAR(); LD_CH(0); LD_CH(1); SBAR();

#define LD_CH2(c) do { LD_CH(c); const unsigned kc_ = KC(c), sl_ = L0 + ((c) % 5) * SLOT; dma16(sX, vX0 - 64 * XROW, kc_ * 128, sl_ + w4 * 2048); dma16(sX, vX1 - 64 * XROW, kc_ * 128, sl_ + w4 * 2048 + 1024); \
        dma16(sW1, vW1 - 32 * WROW, kc_ * 64 * WROW, sl_ + 16384 + w4 * 1024); } while (0)
    const int xrow = 32 * w4 + r, xf = (xrow >> 1) & 7;
    const LAS char* xa[4]; const LAS char* xa3[4];
#pragma unroll
    for (int s = 0; s < 4; ++s) { xa[s] = lds + xrow * 128 + (((2 * s + hh) ^ xf) << 4); xa3[s] = xa[s] + 3 * SLOT; OPQ(xa3[s]); }
#define XFR(c, s) ldsr(((c) % 5 >= 3 ? xa3[s] : xa[s]) + ((c) % 5 >= 3 ? (c) % 5 - 3 : (c) % 5) * SLOT)
    const int q4 = (lane & 15) >> 2;
    const unsigned trl = ((lane >> 4) & 1) * 32 + (lane & 3) * 8;
    const LAS char* wk0 = lds + (8 * hh + q4) * 128 + trl + (q4 >> 1) * 64;
    const LAS char* wk1 = lds + (8 * hh + q4) * 128 + trl + ((q4 >> 1) ^ 1) * 64;
#define WF(b, s) tr2((b) + (s) * 2048, (b) + (s) * 2048 + 512)
#define GEMM2(off, KT) do { const LAS char* b0_ = wk0 + (off); const LAS char* b1_ = wk1 + (off); OPQ(b0_); OPQ(b1_); u32x4 w_[D2 + 1][2]; \
        _Pragma("unroll") for (int s = 0; s < D2; ++s) { w_[s][0] = WF(b0_, s); w_[s][1] = WF(b1_, s); } \
        _Pragma("unroll") for (int s = 0; s < 28; ++s) { if (s + D2 < 28) { w_[(s + D2) % (D2 + 1)][0] = WF(b0_, s + D2); w_[(s + D2) % (D2 + 1)][1] = WF(b1_, s + D2); } SBAR(); \
            if (KT) { a0 = mfma(w_[s % (D2 + 1)][0], xr[s], a0); a1 = mfma(w_[s % (D2 + 1)][1], xr[s], a1); } else { a0 = mfma(xr[s], w_[s % (D2 + 1)][0], a0); a1 = mfma(xr[s], w_[s % (D2 + 1)][1], a1); } SBAR(); } } while (0)
#define GEMM1(acc, wb, KT) do { u32x4 w_[D1 + 1]; \
        _Pragma("unroll") for (int s = 0; s < D1; ++s) w_[s] = tr2((wb) + s * 1024, (wb) + s * 1024 + 256); \
        _Pragma("unroll") for (int s = 0; s < 28; ++s) { if (s + D1 < 28) w_[(s + D1) % (D1 + 1)] = tr2((wb) + (s + D1) * 1024, (wb) + (s + D1) * 1024 + 256); SBAR(); \
            acc = KT ? mfma(w_[s % (D1 + 1)], xr[s], acc) : mfma(xr[s], w_[s % (D1 + 1)], acc); SBAR(); } } while (0)
#define PUB_K(base, g7) do { const unsigned vo_ = (base) + tile * KTILE + (g7) * 4096 + lane * 16; \
        STB(pack8n(a0, 0), rWS, vo_, 0); STB(pack8n(a0, 1), rWS, vo_ + 1024, 0); STB(pack8n(a1, 0), rWS, vo_ + 2048, 0); STB(pack8n(a1, 1), rWS, vo_ + 3072, 0); } while (0)
#define PUB_V(base, g7) do { const unsigned tb_ = (base) + tile * VTILE, vo_ = tb_ + hh * 7168 + (64 * (g7) + r) * 16; \
        STB(pack8(a0, 0), rWS, vo_, 0); STB(pack8(a0, 1), rWS, vo_ + 14336, 0); STB(pack8(a1, 0), rWS, vo_ + 512, 0); STB(pack8(a1, 1), rWS, vo_ + 14848, 0); \
        const unsigned c_ = lane == 0 ? 0x3F803F80u : 0u; STB(((u32x4){c_, c_, c_, c_}), rWS, lane < 8 ? tb_ + 28672 + (g7) * 128 + lane * 16 : WS_END, 0); } while (0)
    const unsigned vVl0 = r < 28 ? hh * 7168 + (28 * h + r) * 16 : 28672 + gc * 128 + (r - 28) * 16, vVl1 = r < 28 ? vVl0 + 14336 : vVl0;
    u64 tag;
#define READ_TAG() do { asm volatile("s_waitcnt lgkmcnt(0)\n\ts_mov_b64 %0, %1" : "=s"(tag) : "s"(tag_raw) : "memory"); tag = tag * 0x9E3779B97F4A7C15ull + 0x632BE59BD9B4E019ull; } while (0)

    const int ux = h == 7 ? 4 : h == 8 ? 5 : h == 10 ? 6 : h == 12 ? 7 : h >= 14 ? h - 6 : -1;
    const unsigned vW2 = (16 * w4 + (lane >> 3)) * WROW + vsw;
#define LD_W2(g, dst) do { _Pragma("unroll") for (int c = 0; c < 7; ++c) { dma16(sWB, vW2, KC(c) * 64 * WROW + (g) * 128, L0 + (dst) + c * 8192 + w4 * 2048); \
            dma16(sWB, vW2, (KC(c) * 64 + 8) * WROW + (g) * 128, L0 + (dst) + c * 8192 + w4 * 2048 + 1024); } } while (0)
    if (wave < 4) {
        dma4(sWB, ((idx & 7) * 56 + w4 * 14 + min(lane & 31, 13)) * WROW + (hh && ux >= 0 ? 14 + (ux >> 1) : h) * 128, 0, L0 + JUNK);
        if (threadIdx.x < 16) *(LAS unsigned*)(lds + CNT + threadIdx.x * 4) = 0u;
        READ_TAG();
        f32x16 a0 = {}, a1 = {};
        u32x4 fx[2][4], fw[2][8];
#define RD_KV(c) do { const LAS char* b0_ = wk0 + ((c) % 5) * SLOT + 16384; const LAS char* b1_ = wk1 + ((c) % 5) * SLOT + 16384; OPQ(b0_); OPQ(b1_); \
            _Pragma("unroll") for (int s = 0; s < 4; ++s) { fx[(c) & 1][s] = XFR(c, s); fw[(c) & 1][2 * s] = WF(b0_, s); fw[(c) & 1][2 * s + 1] = WF(b1_, s); } SBAR(); } while (0)
#define MM_KV(c, KT) do { _Pragma("unroll") for (int s = 0; s < 4; ++s) { \
            if (KT) { a0 = mfma(fw[(c) & 1][2 * s], fx[(c) & 1][s], a0); a1 = mfma(fw[(c) & 1][2 * s + 1], fx[(c) & 1][s], a1); } \
            else { a0 = mfma(fx[(c) & 1][s], fw[(c) & 1][2 * s], a0); a1 = mfma(fx[(c) & 1][s], fw[(c) & 1][2 * s + 1], a1); } } SBAR(); } while (0)
#define PH1A(KT) do { \
        wait_vm<4>(); bar(); RD_KV(0); \
        wait_vm<1>(); bar(); RD_KV(1); MM_KV(0, KT); \
        bar(); RD_KV(2); MM_KV(1, KT); \
        bar(); RD_KV(3); MM_KV(2, KT); \
        bar(); RD_KV(4); MM_KV(3, KT); \
        bar(); RD_KV(5); MM_KV(4, KT); \
        bar(); RD_KV(6); MM_KV(5, KT); MM_KV(6, KT); } while (0)
#define RAISE1() do { cnt_add(lds + C_ST, lane); wait_vm<0>(); if (lane < (h < 14 ? 1 : 2)) __hip_atomic_store(FGP(tile, h < 14 ? h : 2 * h + 10 + lane), tag, RLX_AGENT); } while (0)
        if (h < 7) { PH1A(true); PUB_K(WS_KA, h); RAISE1(); }
        else { PH1A(false); PUB_V(h < 14 ? WS_VA : WS_VB, h < 14 ? h - 7 : h - 9); RAISE1(); }

        PRI(3);
        const GAS u64* fp = FGP(((rb + lane) & 15) * 4 + w4, (lane >> 4) == 0 ? ga : (lane >> 4) == 1 ? gb : (lane >> 4) == 2 ? 7 + gc : 7 + gd);
        unsigned rdy = 0; u64 fl;
#define COUNT() do { u64 m_ = __ballot(fl == tag); m_ &= m_ >> 32; m_ &= m_ >> 16; rdy = (unsigned)__builtin_ctz(~(unsigned)m_ | 0x10000u); } while (0)
#define NEED(n) do { _Pragma("unroll 1") for (unsigned sp = 0; __builtin_expect(rdy < (n), 0) && sp < kSpin; ++sp) { if (sp) __builtin_amdgcn_s_sleep(4); fl = __hip_atomic_load(fp, RLX_AGENT); COUNT(); } asm volatile("" ::: "memory"); } while (0)
        cnt_wait(lds + C_Q, 4);
        u32x4 q[4][2];
#pragma unroll
        for (int t = 0; t < 4; ++t) { const LAS char* p = lds + QX + ((w4 + 1 + t) & 3) * 2048 + lane * 16; q[t][0] = ldsr(p); q[t][1] = ldsr(p + 1024); }
        const float gq = *(const LAS float*)(lds + GBUF + (32 * w4 + r) * 4);

        NEED(3u);

        const unsigned vK = WS_KA + w4 * KTILE + o0 * 512 + lane * 16, vV0 = WS_VA + w4 * VTILE + vVl0, vV1 = WS_VA + w4 * VTILE + vVl1;
        u32x4 kv[4][4];
#define LD_KV(b, i) do { const unsigned g_ = (unsigned)(rb + (i)) & 15u, sk_ = g_ * (4 * KTILE), sv_ = g_ * (4 * VTILE); \
            kv[b][0] = LDB(rWS, vK, sk_); kv[b][1] = LDB(rWS, vK + 1024, sk_); kv[b][2] = LDB(rWS, vV0, sv_); kv[b][3] = LDB(rWS, vV1, sv_); SBAR(); } while (0)
#define KFIX(b) do { kv[b][0][0] = mO ? 0x3F80u : kv[b][0][0]; kv[b][1][2] = mE ? 0x3F80u : kv[b][1][2]; } while (0)
        LD_KV(0, 0); fl = __hip_atomic_load(fp, RLX_AGENT); LD_KV(1, 1); LD_KV(2, 2);
        cnt_add(lds + C_FL, lane);
        kv[3][3] = (u32x4){0u, 0u, 0u, 0u};
        KFIX(0);
#pragma unroll
        for (int t = 0; t < 4; ++t) {
            f32x16 S = mfma(kv[0][0], q[t][0], (f32x16){}); S = mfma(kv[0][1], q[t][1], S);
            const unsigned mb = cvtpk(swap_max(max16(S)), 0.f) & 0xffffu;
            if (!hh) *(LAS unsigned*)(lds + MREF + ((w4 + 1 + t) & 3) * 512 + t * 128 + r * 4) = mb << 16;
            q[t][0][0] = mO ? mb ^ 0x8000u : q[t][0][0]; q[t][1][2] = mE ? mb ^ 0x8000u : q[t][1][2];
        }
        cnt_add(lds + C_QR, lane);

        PRI(1);
        f32x16 O[4] = {};
        f32x16 Sa = mfma(kv[0][0], q[0][0], (f32x16){}), Sb;
        Sa = mfma(kv[0][1], q[0][1], Sa);
        u32x4 pp1 = {0u, 0u, 0u, 0u};
#define EX4(Sx, i) do { Sx[i] = ex2(Sx[i]); Sx[i + 1] = ex2(Sx[i + 1]); Sx[i + 2] = ex2(Sx[i + 2]); Sx[i + 3] = ex2(Sx[i + 3]); } while (0)
#define SUB(Sx, b, t, Sn, bn, tn, bp, tp, E0, E1, E2, E3, EV) do { u32x4 p0_; \
            SBAR(); Sn = mfma(kv[bn][0], q[tn][0], (f32x16){}); SBAR(); E0; SBAR(); EX4(Sx, 0); SBAR(); Sn = mfma(kv[bn][1], q[tn][1], Sn); SBAR(); E1; SBAR(); EX4(Sx, 4); SBAR(); \
            O[tp] = mfma(kv[bp][3], pp1, O[tp]); SBAR(); E2; SBAR(); p0_ = pack8(Sx, 0); EV; SBAR(); EX4(Sx, 8); SBAR(); O[t] = mfma(kv[b][2], p0_, O[t]); SBAR(); E3; SBAR(); \
            EX4(Sx, 12); pp1 = pack8(Sx, 1); SBAR(); } while (0)
#define WVM(n) __builtin_amdgcn_s_waitcnt(0x0F70 | ((n) & 15) | (((n) >> 4) << 14))
#define KT(b) do { \
            SUB(Sa, b, 0, Sb, b, 1, ((b) + 3) & 3, 3, gn = (gn + 1) & 15u, sk = gn * (4 * KTILE), kv[((b) + 3) & 3][0] = LDB(rWS, vK, sk), kv[((b) + 3) & 3][1] = LDB(rWS, vK + 1024, sk), ); \
            SUB(Sb, b, 1, Sa, b, 2, b, 0, sv = gn * (4 * VTILE), kv[((b) + 3) & 3][2] = LDB(rWS, vV0, sv), kv[((b) + 3) & 3][3] = LDB(rWS, vV1, sv), , ); \
            SUB(Sa, b, 2, Sb, b, 3, b, 1, , , WVM(8), , KFIX(((b) + 1) & 3)); \
            SUB(Sb, b, 3, Sa, ((b) + 1) & 3, 0, b, 2, , , , , ); } while (0)
#define KT1(b, n) do { \
            SUB(Sa, b, 0, Sb, b, 1, ((b) + 3) & 3, 3, , , , , ); \
            SUB(Sb, b, 1, Sa, b, 2, b, 0, , , , , ); \
            SUB(Sa, b, 2, Sb, b, 3, b, 1, , , , , KFIX(((b) + 1) & 3)); \
            if (rdy < 16u) { COUNT(); NEED(n); fl = __hip_atomic_load(fp, RLX_AGENT); } LD_KV(((b) + 3) & 3, (b) + 3); \
            SUB(Sb, b, 3, Sa, ((b) + 1) & 3, 0, b, 2, , , , , ); } while (0)
        KT1(0, 4u); KT1(1, 5u); KT1(2, 6u); KT1(3, 7u);
        if (rdy < 16u) { COUNT(); NEED(16u); }
        WVM(8);
        unsigned gn = (unsigned)(rb + 6) & 15u, sk, sv;
        u32x2 pre = {0u, 0u};
#pragma unroll 1
        for (int i = 0; i < 3; ++i) { pre = *(volatile LAS u32x2*)(lds + C_QR); KT(0); KT(1); KT(2); KT(3); }
        O[3] = mfma(kv[3][3], pp1, O[3]);

        PRI(3);
        if (__builtin_expect(__builtin_amdgcn_readfirstlane(min(pre[0], pre[1])) < 4u, 0)) { cnt_wait(lds + C_QR, 4); cnt_wait(lds + C_CLR, 4); }
        asm volatile("" ::: "memory");
        float f[4];
#pragma unroll
        for (int t = 0; t < 4; ++t) f[t] = *(const LAS float*)(lds + MREF + w4 * 512 + t * 128 + r * 4);
#pragma unroll
        for (int t = 0; t < 3; ++t) {
            LAS char* d = lds + MG + (((w4 + 1 + t) & 3) * 3 + t) * MGT;
#pragma unroll
            for (int i = 0; i < 3; ++i) *(LAS f32x4*)(d + i * 1024 + lane * 16) = (f32x4){O[t][4 * i], O[t][4 * i + 1], O[t][4 * i + 2], O[t][4 * i + 3]};
            if (hh) *(LAS float*)(d + 3584 + r * 4) = O[t][12];
            else *(LAS f32x4*)(d + 3072 + r * 16) = (f32x4){O[t][12], O[t][13], O[t][14], O[t][15]};
        }
        cnt_add(lds + C_MG, lane);
        {   const float mm = fmaxf(fmaxf(f[0], f[1]), fmaxf(f[2], f[3]));
#pragma unroll
            for (int t = 0; t < 4; ++t) f[t] = ex2(f[t] - mm); }
        const float lo = from_hi(O[3][12]) * f[3];
        asm volatile("" :: "v"(f[0]), "v"(f[1]), "v"(f[2]), "v"(lo));
        cnt_wait(lds + C_MG, 4);
        f32x16 Oo;
        bool bad;
        {   float pl[3]; f32x4 pt[3][4];
            const LAS char* s = lds + MG + w4 * 3 * MGT;
#pragma unroll
            for (int t = 0; t < 3; ++t) pl[t] = *(const LAS float*)(s + t * MGT + 3584 + r * 4);
#pragma unroll
            for (int t = 0; t < 3; ++t) {
#pragma unroll
                for (int i = 0; i < 3; ++i) pt[t][i] = *(const LAS f32x4*)(s + t * MGT + i * 1024 + lane * 16);
                pt[t][3] = *(const LAS f32x4*)(s + t * MGT + 3072 + r * 16); }
            SBAR();
            const float l = lo + pl[0] * f[0] + pl[1] * f[1] + pl[2] * f[2], sc = gq * __builtin_amdgcn_rcpf(l);
#pragma unroll
            for (int t = 0; t < 4; ++t) f[t] *= sc;
#pragma unroll
            for (int i = 0; i < 16; ++i) Oo[i] = O[3][i] * f[3] + pt[0][i >> 2][i & 3] * f[0] + pt[1][i >> 2][i & 3] * f[1] + pt[2][i >> 2][i & 3] * f[2];
            const float z = (((Oo[0] + Oo[1]) + (Oo[2] + Oo[3])) + ((Oo[4] + Oo[5]) + (Oo[6] + Oo[7]))) + (((Oo[8] + Oo[9]) + (Oo[10] + Oo[11])) + ((Oo[12] + Oo[13]) + (Oo[14] + Oo[15])));
            bad = !(l <= 1.0e30f) || !(__builtin_fabsf(z) <= 3.0e38f);
        }
        if (__builtin_expect(__any(bad), 0)) {
            u32x4 qa = q[3][0], qb = q[3][1];
            if (mO) qa[0] = 0u;
            if (mE) qb[2] = 0u;
            Oo = (f32x16){}; float m = -__builtin_inff();
#pragma unroll 1
            for (int tt = 0; tt < 64; ++tt) {
                const unsigned ko = WS_KA + o0 * 512 + lane * 16;
                const u32x4 k0 = LDB(rWS, ko, tt * KTILE), k1 = LDB(rWS, ko + 1024, tt * KTILE), v0 = LDB(rWS, WS_VA + vVl0, tt * VTILE), v1 = LDB(rWS, WS_VA + vVl1, tt * VTILE);
                f32x16 S = mfma(k0, qa, (f32x16){}); S = mfma(k1, qb, S);
                const float mn = fmaxf(m, swap_max(max16(S))), al = ex2(m - mn);
                m = mn;
#pragma unroll
                for (int i = 0; i < 16; ++i) { S[i] = ex2(S[i] - mn); Oo[i] *= al; }
                Oo = mfma(v0, pack8(S, 0), Oo); Oo = mfma(v1, pack8(S, 1), Oo);
            }
            const float sc = gq / from_hi(Oo[12]);
#pragma unroll
            for (int i = 0; i < 16; ++i) Oo[i] *= sc;
        }
        u32x4 f0 = pack8(Oo, 0), f1 = pack8(Oo, 1);
        if (hh) { f1[2] = 0u; f1[3] = 0u; }
#pragma unroll
        for (int i = 0; i < 4; ++i) { f0[i] = min(f0[i], 0xFFFFFFFEu); f1[i] = min(f1[i], 0xFFFFFFFEu); }
        const unsigned vOA = WS_OA + (tile * 16 + h) * 2048 + lane * 16;
        STB(f0, rWS, vOA, 0); STB(f1, rWS, vOA + 1024, 0);
        cnt_add(lds + C_PUB, lane);

    } else {
        PRI(2);
        LD_CH2(2);
        READ_TAG();
        u32x4 xr[28];
#define GETX(c) do { _Pragma("unroll") for (int s = 0; s < 4; ++s) xr[4 * (c) + s] = XFR(c, s); } while (0)
        wait_vm<9>(); bar(); LD_CH2(3); GETX(0);
        wait_vm<12>(); bar(); LD_CH2(4); GETX(1);
        wait_vm<12>(); bar(); LD_CH2(5); GETX(2);
        wait_vm<12>(); bar(); LD_CH2(6); GETX(3);
        wait_vm<12>(); bar();
        if (lane < 56) dma16(mksrd(gw, XROW), lane * 16, 0, L0 + GATE);
        { const unsigned vWq = (16 * w4 + (lane >> 2)) * WROW + (lane & 3) * 16 + o0 * 16;
#pragma unroll
          for (int c = 0; c < 7; ++c) dma16(sWA, vWq, KC(c) * 64 * WROW, L0 + WQ + c * 4096 + w4 * 1024); }
        GETX(4);
        wait_vm<14>(); bar(); GETX(5);
        wait_vm<8>(); bar(); LD_W2(h, W2); GETX(6);
        wait_vm<14>();
#pragma unroll
        for (int i = 0; i < 2; ++i) { const int c = 4 * i + (lane >> 4);
            if (c < 7) *(LAS unsigned short*)(lds + WQ + c * 4096 + (16 * w4 + (lane & 15)) * 64 + (odd ? 0 : 56)) = *(const LAS unsigned short*)(lds + GATE + (KC(c) * 64 + 16 * w4 + (lane & 15)) * 2); }
        unsigned hs = 0;
#define HSYNC() do { cnt_add(lds + C_H, lane); hs += 4; cnt_wait<2>(lds + C_H, hs); } while (0)
        HSYNC();

        float gq;
        {   f32x16 aQ = {};
            const LAS char* wb = lds + WQ + (8 * hh + q4) * 64 + trl; OPQ(wb);
            GEMM1(aQ, wb, true);
            gq = 1.f / (1.f + __expf(-(odd ? from_lo(aQ[0]) : from_hi(aQ[12]))));
#pragma unroll
            for (int i = 0; i < 16; ++i) aQ[i] *= QSCALE;
            u32x4 q0 = pack8n(aQ, 0), q1 = pack8n(aQ, 1);
            if (mO) { q0[0] = 0u; q0[1] = 0u; }
            if (mE) { q1[2] = 0u; q1[3] = 0u; }
            ldsw(lds + QX + w4 * 2048 + lane * 16, q0); ldsw(lds + QX + w4 * 2048 + 1024 + lane * 16, q1);
            if (!hh) *(LAS float*)(lds + GBUF + (32 * w4 + r) * 4) = gq;
            cnt_add(lds + C_Q, lane);
        }

        const bool proj = (h & 7) < 7;
        const unsigned vO = (8 * w4 + (lane >> 3)) * XROW + (h & 7) * 128 + vsw;
#define LD_WO(p, dst) do { const u32x4 sO_ = mksrd(p, DM * XROW); _Pragma("unroll") for (int i = 0; i < 14; ++i) { wait_vm<WIN - 1>(); dma16l(sO_, vO, i * 32 * XROW, L0 + (dst) + i * 4096 + w4 * 1024); } } while (0)
        u32x4 pb[4];
#define ST_V(g7) do { const unsigned tb = WS_VB + tile * VTILE, vo = tb + hh * 7168 + (64 * (g7) + r) * 16; \
            STB(pb[0], rWS, vo, 0); STB(pb[1], rWS, vo + 14336, 0); STB(pb[2], rWS, vo + 512, 0); STB(pb[3], rWS, vo + 14848, 0); \
            const unsigned c = lane == 0 ? 0x3F803F80u : 0u; STB(((u32x4){c, c, c, c}), rWS, lane < 8 ? tb + 28672 + (g7) * 128 + lane * 16 : WS_END, 0); } while (0)
        {   f32x16 a0 = {}, a1 = {};
            if (ux >= 0) { cnt_wait(lds + C_ST, 4); const unsigned vU = (16 * w4 + (lane >> 2)) * WROW + (lane & 3) * 16 + 1792 + ux * 64;
#pragma unroll
                for (int c = 0; c < 7; ++c) dma16(sWB, vU, KC(c) * 64 * WROW, L0 + c * 4096 + w4 * 1024); }
            wait_vm<0>(); HSYNC();
            if (h < 14) {
                GEMM2(W2, true);
                if (h < 7) {
#pragma unroll
                    for (int i = 0; i < 16; ++i) { a0[i] *= QSCALE; a1[i] *= QSCALE; } }
                pb[0] = pack8n(a0, 0); pb[1] = pack8n(a0, 1); pb[2] = pack8n(a1, 0); pb[3] = pack8n(a1, 1);
            } else {
                GEMM2(W2, false);
                pb[0] = pack8(a0, 0); pb[1] = pack8(a0, 1); pb[2] = pack8(a1, 0); pb[3] = pack8(a1, 1);
            }
            if (ux >= 0) {
                a0 = (f32x16){};
                const LAS char* wb = lds + (8 * hh + q4) * 64 + trl;
                GEMM1(a0, wb, false);
            }

            cnt_wait<2>(lds + C_FL, 4);
            if (h < 14) { const unsigned vo = (h < 7 ? WS_QB + h * 4096 : WS_KB + (h - 7) * 4096) + tile * KTILE + lane * 16;
                STB(pb[0], rWS, vo, 0); STB(pb[1], rWS, vo + 1024, 0); STB(pb[2], rWS, vo + 2048, 0); STB(pb[3], rWS, vo + 3072, 0);
            } else ST_V(h - 14);
            if (ux >= 0) { const unsigned tb = WS_VB + tile * VTILE, vo = tb + hh * 7168 + (32 * ux + r) * 16;
                STB(pack8(a0, 0), rWS, vo, 0); STB(pack8(a0, 1), rWS, vo + 14336, 0);
                const unsigned c = lane == 0 ? 0x3F803F80u : 0u; STB(((u32x4){c, c, c, c}), rWS, lane < 8 ? tb + 28672 + (ux >> 1) * 128 + lane * 16 : WS_END, 0); }
        }
        cnt_add(lds + C_H, lane); hs += 4;
        { const u32x4 ones = {~0u, ~0u, ~0u, ~0u}; const unsigned vo = WS_OA + (tile * 16 + h) * 2048 + lane * 16;
          STB(ones, rWS, vo, 0); STB(ones, rWS, vo + 1024, 0); }
        wait_vm<0>();
        if (lane == 0 || (lane == 1 && h >= 14) || (lane == 2 && ux >= 0)) __hip_atomic_store(FGP(tile, lane == 2 ? 28 + ux : h < 14 ? 14 + h : 2 * h + lane), tag, RLX_AGENT);
        cnt_add(lds + C_CLR, lane);

        cnt_wait<2>(lds + C_H, hs);
        if (proj) LD_WO(woB, WOB);
        const int rt = w4 & 1, kh = w4 >> 1, T = rb * 4 + (h >> 3) * 2 + rt;
        const LAS char* wo0 = lds + kh * 28672 + (4 * hh + q4) * 128 + trl + (q4 >> 1) * 64;
        const LAS char* wo1 = lds + kh * 28672 + (4 * hh + q4) * 128 + trl + ((q4 >> 1) ^ 1) * 64;
        const unsigned wfix = 3072 - hh * 1024;
#define WOF(b, bx, hd, s) ((s) == 0 ? tr2((b) + (hd) * 3584, (b) + (hd) * 3584 + 1024) : tr2((b) + (hd) * 3584 + 2048, (bx) + (hd) * 3584))
        {   const int i = lane & 7, g = i == 0 ? 14 + ga : i == 1 ? 14 + gb : i == 2 ? 21 + ga : i == 3 ? 21 + gb : i == 4 ? 28 + ((28 * h) >> 5) : 28 + ((28 * h + 27) >> 5);
            const int tp = tile > 0 ? tile - 1 : 0;
            const int tf = (lane & 8) ? tp : tile;
            poll_flags<24>(FGP(tf, g), tag);

            const unsigned vk = o0 * 512 + lane * 16;
            u32x4 b0 = LDB(rWS, vk, WS_QB + tile * KTILE), b1 = LDB(rWS, vk + 1024, WS_QB + tile * KTILE);
            const u32x4 kp0 = LDB(rWS, vk, WS_KB + tp * KTILE), kp1 = LDB(rWS, vk + 1024, WS_KB + tp * KTILE), ko0 = LDB(rWS, vk, WS_KB + tile * KTILE), ko1 = LDB(rWS, vk + 1024, WS_KB + tile * KTILE);
            const u32x4 vp1 = LDB(rWS, vVl1, WS_VB + tp * VTILE), vo0 = LDB(rWS, vVl0, WS_VB + tile * VTILE), vo1 = LDB(rWS, vVl1, WS_VB + tile * VTILE);
            if (mO) { b0[0] = 0u; b0[1] = 0u; }
            if (mE) { b1[2] = 0u; b1[3] = 0u; }
            f32x16 A = mfma(kp0, b0, (f32x16){}); A = mfma(kp1, b1, A);
            f32x16 B = mfma(ko0, b0, (f32x16){}); B = mfma(ko1, b1, B);
            const u64 M = 0x5555ull << (18 + r);
            unsigned Mp = (unsigned)M >> (4 * hh); const unsigned Mo = (unsigned)(M >> 32) >> (4 * hh);
            if (tile == 0) Mp = 0u;
#pragma unroll
            for (int i2 = 0; i2 < 16; ++i2) { const int c = (i2 & 3) + 8 * (i2 >> 2);
                const unsigned tb = (unsigned)__builtin_amdgcn_sbfe((int)Mo, c, 1); B[i2] = __uint_as_float((__float_as_uint(B[i2]) & tb) | (0xff800000u & ~tb));
                if (i2 >= 8) { const unsigned ta = (unsigned)__builtin_amdgcn_sbfe((int)Mp, c, 1); A[i2] = __uint_as_float((__float_as_uint(A[i2]) & ta) | (0xff800000u & ~ta)); } }
            float m = max16(B);
            m = MX3(m, A[8], A[9]); m = MX3(m, A[10], A[11]); m = MX3(m, A[12], A[13]); m = MX3(m, A[14], A[15]);
            m = swap_max(m);
#pragma unroll
            for (int i2 = 0; i2 < 16; ++i2) { B[i2] = ex2(B[i2] - m); if (i2 >= 8) A[i2] = ex2(A[i2] - m); }
            f32x16 OB = mfma(vp1, pack8(A, 1), (f32x16){});
            OB = mfma(vo0, pack8(B, 0), OB); OB = mfma(vo1, pack8(B, 1), OB);
            const float sc = (1.f - gq) * __builtin_amdgcn_rcpf(from_hi(OB[12]));
#pragma unroll
            for (int i2 = 0; i2 < 16; ++i2) OB[i2] *= sc;
            u32x4 f0 = pack8(OB, 0), f1 = pack8(OB, 1);
            if (hh) { f1[2] = 0u; f1[3] = 0u; }
            const unsigned vo = WS_OB + (tile * 16 + h) * 2048 + lane * 16;
            STB(f0, rWS, vo, 0); STB(f1, rWS, vo + 1024, 0);
        }
        wait_vm<0>();
        if (lane == 0) __hip_atomic_store((GAS u64*)(ws + WS_FOB + (tile * 16 + h) * 8), tag, RLX_AGENT);

        if (proj) {
            cnt_add(lds + C_H, lane); hs += 4;
            LD_WO(woA, WOA);
            poll_flags<24>((const GAS u64*)(ws + WS_FOB + (T * 16 + 8 * kh + (lane & 7)) * 8), tag);
            cnt_wait<2>(lds + C_H, hs);
            f32x16 c0 = {}, c1 = {};
            const unsigned vF = T * 32768 + kh * 16384 + lane * 16;
            {   const LAS char* p0x = wo0 + wfix; const LAS char* p1x = wo1 + wfix;
                u32x4 of[16];
                SBAR(); PRI(0);
#pragma unroll
                for (int i = 0; i < 16; ++i) of[i] = LDB(rWS, vF, WS_OB + i * 1024);
                SBAR(); PRI(2);
#pragma unroll
                for (int i = 0; i < 16; ++i) { c0 = mfma(WOF(wo0, p0x, i >> 1, i & 1), of[i], c0); c1 = mfma(WOF(wo1, p1x, i >> 1, i & 1), of[i], c1); SBAR(); }
            }
            wait_vm<0>(); HSYNC();
            u32x4 wf[8][2][2];
            {   const LAS char* p0 = wo0 + WOA; const LAS char* p1 = wo1 + WOA; const LAS char* p0x = p0 + wfix; const LAS char* p1x = p1 + wfix; OPQ(p0); OPQ(p1); OPQ(p0x); OPQ(p1x);
#pragma unroll
                for (int hd = 0; hd < 8; ++hd) { wf[hd][0][0] = WOF(p0, p0x, hd, 0); wf[hd][1][0] = WOF(p0, p0x, hd, 1); wf[hd][0][1] = WOF(p1, p1x, hd, 0); wf[hd][1][1] = WOF(p1, p1x, hd, 1); }
            }

            cnt_wait<4>(lds + C_PUB, 4);
            __builtin_amdgcn_s_sleep(8);

            unsigned nx = 0;
#pragma unroll 1
            for (unsigned sp = 0; sp < kSpin && nx < 8; ++sp) {
                asm volatile("" ::: "memory");
                u32x4 of[16];
#pragma unroll
                for (int i = 0; i < 16; ++i) of[i] = LDB(rWS, vF, (unsigned)(i >> 1) >= nx ? WS_OA + i * 1024 : WS_END);
#pragma unroll
                for (int hd = 0; hd < 8; ++hd) {
                    const u32x4 a = of[2 * hd], b = of[2 * hd + 1];
                    const unsigned mx = max(max(max(a[0], a[1]), max(a[2], a[3])), max(max(b[0], b[1]), max(b[2], b[3])));
                    const unsigned ok = (unsigned)__all(mx != ~0u) | (unsigned)(sp + 1 == kSpin);
                    if ((unsigned)(nx == (unsigned)hd) & ok) {
                        c0 = mfma(wf[hd][0][0], a, c0); c1 = mfma(wf[hd][0][1], a, c1); c0 = mfma(wf[hd][1][0], b, c0); c1 = mfma(wf[hd][1][1], b, c1);
                        nx = hd + 1;
                    }
                }
            }
            const f32x16 give = kh ? c0 : c1, keep = kh ? c1 : c0;
            { LAS char* d = lds + WOB + w4 * 4096 + lane * 16;
#pragma unroll
              for (int i = 0; i < 4; ++i) *(LAS f32x4*)(d + i * 1024) = (f32x4){give[4 * i], give[4 * i + 1], give[4 * i + 2], give[4 * i + 3]}; }
            cnt_add(lds + C_PAIR + rt * 4, lane); cnt_wait(lds + C_PAIR + rt * 4, 2);
            f32x16 fin;
            { const LAS char* s = lds + WOB + (w4 ^ 2) * 4096 + lane * 16;
#pragma unroll
              for (int i = 0; i < 4; ++i) { const f32x4 t = *(const LAS f32x4*)(s + i * 1024);
#pragma unroll
                  for (int k = 0; k < 4; ++k) fin[4 * i + k] = keep[4 * i + k] + t[k]; } }
            GAS char* orow = (GAS char*)out + (T * 32 + r) * XROW + (h & 7) * 128 + kh * 64 + hh * 16;
            *(GAS u32x4*)orow = pack8n(fin, 0); *(GAS u32x4*)(orow + 32) = pack8n(fin, 1);
        } else if (h == 7 && rb == 0 && wave == 4) {
#pragma unroll 1
            for (int i = 0; i < 4; ++i) { const int hq = i * 4 + (lane >> 4); poll_flags<24>(FGP((lane & 15) * 4, hq < 15 ? 14 + hq : 30), tag); }
            if (lane == 0) __hip_atomic_store((GAS u64*)(ws + WS_EPOCH), tag, RLX_AGENT);
        }
    }
}

extern "C" void kernel_launch(void* const* d_in, const int* in_sizes, int n_in, void* d_out, int out_size, void* d_ws, size_t ws_size, hipStream_t stream) {
    fused_kernel<<<256, 512, 0, stream>>>((const char*)d_in[0], (const char*)d_in[1], (const char*)d_in[2], (const char*)d_in[4], (const char*)d_in[5],
                                          (const char*)d_in[6], (char*)d_out, (char*)d_ws);
}
```

```cpp
#include <hip/hip_runtime.h>
#include <hip/hip_bf16.h>
#include <stdint.h>

typedef __bf16 bf16x8_t __attribute__((ext_vector_type(8)));
typedef __bf16 bf16x2_t __attribute__((ext_vector_type(2)));
typedef float f32x2 __attribute__((ext_vector_type(2)));
typedef float f32x4 __attribute__((ext_vector_type(4)));
typedef float f32x16 __attribute__((ext_vector_type(16)));
typedef unsigned u32x4 __attribute__((ext_vector_type(4)));
typedef unsigned u32x2 __attribute__((ext_vector_type(2)));
typedef short s16x4 __attribute__((ext_vector_type(4)));
typedef unsigned long long u64;
#define LAS __attribute__((address_space(3)))
#define GAS __attribute__((address_space(1)))
#define DI __device__ __forceinline__
#define SBAR() __builtin_amdgcn_sched_barrier(0)
#define PRI(n) __builtin_amdgcn_s_setprio(n)
#define RLX_AGENT __ATOMIC_RELAXED, __HIP_MEMORY_SCOPE_AGENT
#define RLX_WG __ATOMIC_RELAXED, __HIP_MEMORY_SCOPE_WORKGROUP
#define OPQ(p) asm volatile("" : "+v"(p))

constexpr int WIN = 4;
constexpr int D1 = 7, D2 = 3;
constexpr int SEQ = 2048, DM = 448, N3 = 1344;
constexpr int XROW = DM * 2;
constexpr int WROW = N3 * 2;
constexpr float QSCALE = 0.18898223650461362f * 1.4426950408889634f;

constexpr unsigned WS_EPOCH = 0;
constexpr unsigned WS_FG = 4096;
constexpr unsigned WS_FOB = WS_FG + 4 * 42 * 128;
#define FGP(t, gid) ((GAS u64*)(ws + WS_FG + (((t) & 3) * 42 + (gid)) * 128 + ((t) >> 2) * 8))
constexpr unsigned KTILE = 28672;
constexpr unsigned VTILE = 29568;
constexpr unsigned WS_KA = 65536, WS_QB = WS_KA + 64 * KTILE, WS_KB = WS_QB + 64 * KTILE;
constexpr unsigned WS_VA = WS_KB + 64 * KTILE, WS_VB = WS_VA + 64 * VTILE;
constexpr unsigned WS_OA = WS_VB + 64 * VTILE;
constexpr unsigned WS_OB = WS_OA + 2097152;
constexpr unsigned WS_END = WS_OB + 2097152;

constexpr unsigned SLOT = 24576;
constexpr unsigned WQ = 5 * SLOT;
constexpr unsigned QX = WQ + 28672;
constexpr unsigned GATE = QX + 8192;
constexpr unsigned GBUF = GATE + 896;
constexpr unsigned MREF = GBUF + 512;
constexpr unsigned CNT = MREF + 2048;
constexpr unsigned LDS_BYTES = 163840;
constexpr unsigned W2 = 2 * SLOT;
constexpr unsigned WOB = 0, WOA = 57344, MG = 114688, MGT = 3712;
constexpr unsigned C_H = CNT, C_MG = CNT + 8, C_PAIR = CNT + 12, C_ST = CNT + 20, C_FL = CNT + 24, C_Q = CNT + 28, C_QR = CNT + 32, C_CLR = CNT + 36, C_PUB = CNT + 40;
constexpr unsigned JUNK = CNT + 64;
static_assert(JUNK + 256 <= LDS_BYTES && MG + 12 * MGT <= GATE, "LDS map");

DI unsigned cvtpk(float a, float b) { f32x2 v = {a, b}; return __builtin_bit_cast(unsigned, __builtin_convertvector(v, bf16x2_t)); }
DI f32x16 mfma(u32x4 a, u32x4 b, f32x16 c) { return __builtin_amdgcn_mfma_f32_32x32x16_bf16(__builtin_bit_cast(bf16x8_t, a), __builtin_bit_cast(bf16x8_t, b), c, 0, 0, 0); }
DI u32x4 pack8(const f32x16& a, int s) { return (u32x4){cvtpk(a[8 * s], a[8 * s + 1]), cvtpk(a[8 * s + 2], a[8 * s + 3]), cvtpk(a[8 * s + 4], a[8 * s + 5]), cvtpk(a[8 * s + 6], a[8 * s + 7])}; }
DI u32x4 pack8n(const f32x16& a, int s) {
    const u32x4 p = pack8(a, s);
    const auto x = __builtin_amdgcn_permlane32_swap(p[0], p[2], false, false), y = __builtin_amdgcn_permlane32_swap(p[1], p[3], false, false);
    return (u32x4){x[0], y[0], x[1], y[1]};
}
DI u32x4 ldsr(const LAS char* p) { return *(const LAS u32x4*)p; }
DI void ldsw(LAS char* p, u32x4 v) { *(LAS u32x4*)p = v; }
DI u32x2 trr(const LAS char* p) { return __builtin_bit_cast(u32x2, __builtin_amdgcn_ds_read_tr16_b64_v4i16((LAS s16x4*)p)); }
DI u32x4 tr2(const LAS char* p0, const LAS char* p1) { u32x2 a = trr(p0), b = trr(p1); return (u32x4){a[0], a[1], b[0], b[1]}; }
DI float ex2(float x) { return __builtin_amdgcn_exp2f(x); }
DI float swap_max(float m) { auto rr = __builtin_amdgcn_permlane32_swap(__float_as_uint(m), __float_as_uint(m), false, false); return fmaxf(__uint_as_float(rr[0]), __uint_as_float(rr[1])); }
DI float from_hi(float v) { auto rr = __builtin_amdgcn_permlane32_swap(__float_as_uint(v), __float_as_uint(v), false, false); return __uint_as_float(rr[1]); }
DI float from_lo(float v) { auto rr = __builtin_amdgcn_permlane32_swap(__float_as_uint(v), __float_as_uint(v), false, false); return __uint_as_float(rr[0]); }
DI u32x4 mksrd(const void* p, unsigned bytes) { u64 a = (u64)p; return (u32x4){(unsigned)a, (unsigned)(a >> 32) & 0xffffu, bytes, 0x00020000u}; }
DI void dma16(u32x4 srd, unsigned voff, unsigned soff, unsigned ldsaddr) {
    unsigned keep;
    asm volatile("s_mov_b32 %0, m0\n\ts_mov_b32 m0, %4\n\ts_nop 2\n\tbuffer_load_dwordx4 %1, %2, %3 offen lds\n\ts_mov_b32 m0, %0"
                 : "=&s"(keep) : "v"(voff), "s"(srd), "s"(soff), "s"(ldsaddr) : "memory");
}
DI void dma16l(u32x4 srd, unsigned voff, unsigned soff, unsigned ldsaddr) {
    unsigned keep;
    asm volatile("s_mov_b32 %0, m0\n\ts_mov_b32 m0, %4\n\ts_setprio 0\n\ts_nop 1\n\tbuffer_load_dwordx4 %1, %2, %3 offen lds\n\ts_setprio 2\n\ts_mov_b32 m0, %0"
                 : "=&s"(keep) : "v"(voff), "s"(srd), "s"(soff), "s"(ldsaddr) : "memory");
}
DI void dma4(u32x4 srd, unsigned voff, unsigned soff, unsigned ldsaddr) {
    unsigned keep;
    asm volatile("s_mov_b32 %0, m0\n\ts_mov_b32 m0, %4\n\ts_nop 2\n\tbuffer_load_dword %1, %2, %3 offen lds\n\ts_mov_b32 m0, %0"
                 : "=&s"(keep) : "v"(voff), "s"(srd), "s"(soff), "s"(ldsaddr) : "memory");
}
template <int N> DI void wait_vm() { asm volatile("s_waitcnt vmcnt(%0)" ::"n"(N) : "memory"); }
DI void bar() { asm volatile("s_waitcnt lgkmcnt(0)\n\ts_barrier" ::: "memory"); }
constexpr unsigned kSpin = 1u << 14;
DI void cnt_add(LAS char* c, int lane) { asm volatile("s_waitcnt lgkmcnt(0)" ::: "memory"); if (lane == 0) __hip_atomic_fetch_add((LAS unsigned*)c, 1u, RLX_WG); }
template <int SL = 1> DI void cnt_wait(LAS char* c, unsigned target) {
#pragma unroll 1
    for (unsigned sp = 0; sp < kSpin * 128; ++sp) { if (__builtin_expect((unsigned)__builtin_amdgcn_readfirstlane(__hip_atomic_load((LAS unsigned*)c, RLX_WG)) >= target, 1)) break; __builtin_amdgcn_s_sleep(SL); }
    asm volatile("" ::: "memory");
}
template <int SL = 1> DI void poll_flags(const GAS u64* p, u64 tag) {
#pragma unroll 1
    for (unsigned sp = 0; sp < kSpin; ++sp) { if (__all(__hip_atomic_load(p, RLX_AGENT) == tag)) break; __builtin_amdgcn_s_sleep(SL); }
    asm volatile("" ::: "memory");
}
#define MX3(a, b, c) __builtin_fmaxf(__builtin_fmaxf((a), (b)), (c))
DI float max16(const f32x16& p) {
    float a = MX3(p[0], p[1], p[2]), b = MX3(p[3], p[4], p[5]);
    a = MX3(a, p[6], p[7]); b = MX3(b, p[8], p[9]); a = MX3(a, p[10], p[11]); b = MX3(b, p[12], p[13]); a = MX3(a, p[14], p[15]);
    return __builtin_fmaxf(a, b);
}
#define LDB(rs, vo, so) __builtin_amdgcn_raw_buffer_load_b128(rs, vo, so, 16)
#define STB(v, rs, vo, so) __builtin_amdgcn_raw_buffer_store_b128(v, rs, vo, 0, 16)

__global__ __launch_bounds__(512, 2) void fused_kernel(const char* __restrict__ x, const char* __restrict__ wA, const char* __restrict__ woA,
                                                       const char* __restrict__ wB, const char* __restrict__ woB, const char* __restrict__ gw,
                                                       char* __restrict__ out, char* ws) {
    __shared__ __attribute__((aligned(16))) char lds_[LDS_BYTES];
    LAS char* lds = (LAS char*)lds_;
    const unsigned L0 = (unsigned)(size_t)lds;

    u64 tag_raw;
    asm volatile("s_load_dwordx2 %0, %1, 0x0" : "=s"(tag_raw) : "s"(ws) : "memory");
    const int lane = threadIdx.x & 63, r = lane & 31, hh = lane >> 5;
    const int wave = __builtin_amdgcn_readfirstlane(threadIdx.x >> 6), w4 = wave & 3;
    const int xcd = blockIdx.x & 7, idx = blockIdx.x >> 3;
    const int h = (xcd & 3) * 4 + (idx >> 3), rb = (xcd >> 2) * 8 + (idx & 7), rot = ((idx & 7) + 2 * (idx >> 3)) % 7;
    const int r0 = rb * 128, tile = rb * 4 + w4;
    const unsigned kctab = (unsigned)(0x65432106543210ull >> (4 * rot));
#define KC(c) ((kctab >> (4 * (c))) & 7u)
    const int o0 = (7 * h) >> 1;
    const bool odd = h & 1;
    const bool mE = !odd && hh, mO = odd && !hh;
    const int ga = o0 >> 3, gb = (8 * o0 + 31) >> 6, gc = (28 * h) >> 6, gd = (28 * h + 27) >> 6;

    const u32x4 sX = mksrd(x, SEQ * XROW), sWA = mksrd(wA, DM * WROW), sWB = mksrd(wB, DM * WROW);
    const __amdgpu_buffer_rsrc_t rWS = __builtin_amdgcn_make_buffer_rsrc(ws, 0, WS_END, 0x00020000);

    const u32x4 sW1 = h < 14 ? sWA : sWB;
    const unsigned vX0 = (r0 + 16 * wave + (lane >> 3)) * XROW + (((lane & 7) ^ (lane >> 4)) << 4);
    const unsigned vX1 = (r0 + 16 * wave + 8 + (lane >> 3)) * XROW + (((lane & 7) ^ (lane >> 4) ^ 4) << 4);
    const unsigned vsw = ((lane & 7) ^ ((lane >> 2) & 4)) << 4;
    const unsigned vW1 = (8 * wave + (lane >> 3)) * WROW + (h < 14 ? XROW + 128 * h : 128 * (h + 5)) + vsw;
#define LD_CH(c) do { const unsigned kc_ = KC(c), sl_ = L0 + ((c) % 5) * SLOT; dma16(sX, vX0, kc_ * 128, sl_ + wave * 2048); dma16(sX, vX1, kc_ * 128, sl_ + wave * 2048 + 1024); \
        dma16(sW1, vW1, kc_ * 64 * WROW, sl_ + 16384 + wave * 1024); } while (0)
    SBAR(); LD_CH(0); LD_CH(1); SBAR();

#define LD_CH2(c) do { LD_CH(c); const unsigned kc_ = KC(c), sl_ = L0 + ((c) % 5) * SLOT; dma16(sX, vX0 - 64 * XROW, kc_ * 128, sl_ + w4 * 2048); dma16(sX, vX1 - 64 * XROW, kc_ * 128, sl_ + w4 * 2048 + 1024); \
        dma16(sW1, vW1 - 32 * WROW, kc_ * 64 * WROW, sl_ + 16384 + w4 * 1024); } while (0)
    const int xrow = 32 * w4 + r, xf = (xrow >> 1) & 7;
    const LAS char* xa[4]; const LAS char* xa3[4];
#pragma unroll
    for (int s = 0; s < 4; ++s) { xa[s] = lds + xrow * 128 + (((2 * s + hh) ^ xf) << 4); xa3[s] = xa[s] + 3 * SLOT; OPQ(xa3[s]); }
#define XFR(c, s) ldsr(((c) % 5 >= 3 ? xa3[s] : xa[s]) + ((c) % 5 >= 3 ? (c) % 5 - 3 : (c) % 5) * SLOT)
    const int q4 = (lane & 15) >> 2;
    const unsigned trl = ((lane >> 4) & 1) * 32 + (lane & 3) * 8;
    const LAS char* wk0 = lds + (8 * hh + q4) * 128 + trl + (q4 >> 1) * 64;
    const LAS char* wk1 = lds + (8 * hh + q4) * 128 + trl + ((q4 >> 1) ^ 1) * 64;
#define WF(b, s) tr2((b) + (s) * 2048, (b) + (s) * 2048 + 512)
#define GEMM2(off, KT) do { const LAS char* b0_ = wk0 + (off); const LAS char* b1_ = wk1 + (off); OPQ(b0_); OPQ(b1_); u32x4 w_[D2 + 1][2]; \
        _Pragma("unroll") for (int s = 0; s < D2; ++s) { w_[s][0] = WF(b0_, s); w_[s][1] = WF(b1_, s); } \
        _Pragma("unroll") for (int s = 0; s < 28; ++s) { if (s + D2 < 28) { w_[(s + D2) % (D2 + 1)][0] = WF(b0_, s + D2); w_[(s + D2) % (D2 + 1)][1] = WF(b1_, s + D2); } SBAR(); \
            if (KT) { a0 = mfma(w_[s % (D2 + 1)][0], xr[s], a0); a1 = mfma(w_[s % (D2 + 1)][1], xr[s], a1); } else { a0 = mfma(xr[s], w_[s % (D2 + 1)][0], a0); a1 = mfma(xr[s], w_[s % (D2 + 1)][1], a1); } SBAR(); } } while (0)
#define GEMM1(acc, wb, KT) do { u32x4 w_[D1 + 1]; \
        _Pragma("unroll") for (int s = 0; s < D1; ++s) w_[s] = tr2((wb) + s * 1024, (wb) + s * 1024 + 256); \
        _Pragma("unroll") for (int s = 0; s < 28; ++s) { if (s + D1 < 28) w_[(s + D1) % (D1 + 1)] = tr2((wb) + (s + D1) * 1024, (wb) + (s + D1) * 1024 + 256); SBAR(); \
            acc = KT ? mfma(w_[s % (D1 + 1)], xr[s], acc) : mfma(xr[s], w_[s % (D1 + 1)], acc); SBAR(); } } while (0)
#define PUB_K(base, g7) do { const unsigned vo_ = (base) + tile * KTILE + (g7) * 4096 + lane * 16; \
        STB(pack8n(a0, 0), rWS, vo_, 0); STB(pack8n(a0, 1), rWS, vo_ + 1024, 0); STB(pack8n(a1, 0), rWS, vo_ + 2048, 0); STB(pack8n(a1, 1), rWS, vo_ + 3072, 0); } while (0)
#define PUB_V(base, g7) do { const unsigned tb_ = (base) + tile * VTILE, vo_ = tb_ + hh * 7168 + (64 * (g7) + r) * 16; \
        STB(pack8(a0, 0), rWS, vo_, 0); STB(pack8(a0, 1), rWS, vo_ + 14336, 0); STB(pack8(a1, 0), rWS, vo_ + 512, 0); STB(pack8(a1, 1), rWS, vo_ + 14848, 0); \
        const unsigned c_ = lane == 0 ? 0x3F803F80u : 0u; STB(((u32x4){c_, c_, c_, c_}), rWS, lane < 8 ? tb_ + 28672 + (g7) * 128 + lane * 16 : WS_END, 0); } while (0)
    const unsigned vVl0 = r < 28 ? hh * 7168 + (28 * h + r) * 16 : 28672 + gc * 128 + (r - 28) * 16, vVl1 = r < 28 ? vVl0 + 14336 : vVl0;
    u64 tag;
#define READ_TAG() do { asm volatile("s_waitcnt lgkmcnt(0)\n\ts_mov_b64 %0, %1" : "=s"(tag) : "s"(tag_raw) : "memory"); tag = tag * 0x9E3779B97F4A7C15ull + 0x632BE59BD9B4E019ull; } while (0)

    const int ux = h == 7 ? 4 : h == 15 ? 5 : h == 1 ? 6 : h == 5 ? 7 : h == 8 ? 8 : h == 10 ? 9 : -1;
    const unsigned vW2 = (16 * w4 + (lane >> 3)) * WROW + vsw;
#define LD_W2(g, dst) do { _Pragma("unroll") for (int c = 0; c < 7; ++c) { dma16(sWB, vW2, KC(c) * 64 * WROW + (g) * 128, L0 + (dst) + c * 8192 + w4 * 2048); \
            dma16(sWB, vW2, (KC(c) * 64 + 8) * WROW + (g) * 128, L0 + (dst) + c * 8192 + w4 * 2048 + 1024); } } while (0)
    if (wave < 4) {
        dma4(sWB, ((idx & 7) * 56 + w4 * 14 + min(lane & 31, 13)) * WROW + (hh && ux >= 0 ? 14 + (ux >> 1) : h) * 128, 0, L0 + JUNK);
        if (threadIdx.x < 16) *(LAS unsigned*)(lds + CNT + threadIdx.x * 4) = 0u;
        READ_TAG();
        f32x16 a0 = {}, a1 = {};
        u32x4 fx[2][4], fw[2][8];
#define RD_KV(c) do { const LAS char* b0_ = wk0 + ((c) % 5) * SLOT + 16384; const LAS char* b1_ = wk1 + ((c) % 5) * SLOT + 16384; OPQ(b0_); OPQ(b1_); \
            _Pragma("unroll") for (int s = 0; s < 4; ++s) { fx[(c) & 1][s] = XFR(c, s); fw[(c) & 1][2 * s] = WF(b0_, s); fw[(c) & 1][2 * s + 1] = WF(b1_, s); } SBAR(); } while (0)
#define MM_KV(c, KT) do { _Pragma("unroll") for (int s = 0; s < 4; ++s) { \
            if (KT) { a0 = mfma(fw[(c) & 1][2 * s], fx[(c) & 1][s], a0); a1 = mfma(fw[(c) & 1][2 * s + 1], fx[(c) & 1][s], a1); } \
            else { a0 = mfma(fx[(c) & 1][s], fw[(c) & 1][2 * s], a0); a1 = mfma(fx[(c) & 1][s], fw[(c) & 1][2 * s + 1], a1); } } SBAR(); } while (0)
#define PH1A(KT) do { \
        wait_vm<4>(); bar(); RD_KV(0); \
        wait_vm<1>(); bar(); RD_KV(1); MM_KV(0, KT); \
        bar(); RD_KV(2); MM_KV(1, KT); \
        bar(); RD_KV(3); MM_KV(2, KT); \
        bar(); RD_KV(4); MM_KV(3, KT); \
        bar(); RD_KV(5); MM_KV(4, KT); \
        bar(); RD_KV(6); MM_KV(5, KT); MM_KV(6, KT); } while (0)
#define RAISE1() do { cnt_add(lds + C_ST, lane); wait_vm<0>(); if (lane < (h < 14 ? 1 : 2)) __hip_atomic_store(FGP(tile, h < 14 ? h : 2 * h + 10 + lane), tag, RLX_AGENT); } while (0)
        if (h < 7) { PH1A(true); PUB_K(WS_KA, h); RAISE1(); }
        else { PH1A(false); PUB_V(h < 14 ? WS_VA : WS_VB, h < 14 ? h - 7 : h - 9); RAISE1(); }

        PRI(3);
        const GAS u64* fp = FGP(((rb + lane) & 15) * 4 + w4, (lane >> 4) == 0 ? ga : (lane >> 4) == 1 ? gb : (lane >> 4) == 2 ? 7 + gc : 7 + gd);
        unsigned rdy = 0; u64 fl;
#define COUNT() do { u64 m_ = __ballot(fl == tag); m_ &= m_ >> 32; m_ &= m_ >> 16; rdy = (unsigned)__builtin_ctz(~(unsigned)m_ | 0x10000u); } while (0)
#define NEED(n) do { _Pragma("unroll 1") for (unsigned sp = 0; __builtin_expect(rdy < (n), 0) && sp < kSpin; ++sp) { if (sp) __builtin_amdgcn_s_sleep(4); fl = __hip_atomic_load(fp, RLX_AGENT); COUNT(); } asm volatile("" ::: "memory"); } while (0)
        cnt_wait(lds + C_Q, 4);
        u32x4 q[4][2];
#pragma unroll
        for (int t = 0; t < 4; ++t) { const LAS char* p = lds + QX + ((w4 + 1 + t) & 3) * 2048 + lane * 16; q[t][0] = ldsr(p); q[t][1] = ldsr(p + 1024); }
        const float gq = *(const LAS float*)(lds + GBUF + (32 * w4 + r) * 4);

        NEED(3u);

        const unsigned vK = WS_KA + w4 * KTILE + o0 * 512 + lane * 16, vV0 = WS_VA + w4 * VTILE + vVl0, vV1 = WS_VA + w4 * VTILE + vVl1;
        u32x4 kv[4][4];
#define LD_KV(b, i) do { const unsigned g_ = (unsigned)(rb + (i)) & 15u, sk_ = g_ * (4 * KTILE), sv_ = g_ * (4 * VTILE); \
            kv[b][0] = LDB(rWS, vK, sk_); kv[b][1] = LDB(rWS, vK + 1024, sk_); kv[b][2] = LDB(rWS, vV0, sv_); kv[b][3] = LDB(rWS, vV1, sv_); SBAR(); } while (0)
#define KFIX(b) do { kv[b][0][0] = mO ? 0x3F80u : kv[b][0][0]; kv[b][1][2] = mE ? 0x3F80u : kv[b][1][2]; } while (0)
        LD_KV(0, 0); fl = __hip_atomic_load(fp, RLX_AGENT); LD_KV(1, 1); LD_KV(2, 2);
        cnt_add(lds + C_FL, lane);
        kv[3][3] = (u32x4){0u, 0u, 0u, 0u};
        KFIX(0);
#pragma unroll
        for (int t = 0; t < 4; ++t) {
            f32x16 S = mfma(kv[0][0], q[t][0], (f32x16){}); S = mfma(kv[0][1], q[t][1], S);
            const unsigned mb = cvtpk(swap_max(max16(S)), 0.f) & 0xffffu;
            if (!hh) *(LAS unsigned*)(lds + MREF + ((w4 + 1 + t) & 3) * 512 + t * 128 + r * 4) = mb << 16;
            q[t][0][0] = mO ? mb ^ 0x8000u : q[t][0][0]; q[t][1][2] = mE ? mb ^ 0x8000u : q[t][1][2];
        }
        cnt_add(lds + C_QR, lane);

        PRI(1);
        f32x16 O[4] = {};
        f32x16 Sa = mfma(kv[0][0], q[0][0], (f32x16){}), Sb;
        Sa = mfma(kv[0][1], q[0][1], Sa);
        u32x4 pp1 = {0u, 0u, 0u, 0u};
#define EX4(Sx, i) do { Sx[i] = ex2(Sx[i]); Sx[i + 1] = ex2(Sx[i + 1]); Sx[i + 2] = ex2(Sx[i + 2]); Sx[i + 3] = ex2(Sx[i + 3]); } while (0)
#define SUB(Sx, b, t, Sn, bn, tn, bp, tp, E0, E1, E2, E3, EV) do { u32x4 p0_; \
            SBAR(); Sn = mfma(kv[bn][0], q[tn][0], (f32x16){}); SBAR(); E0; SBAR(); EX4(Sx, 0); SBAR(); Sn = mfma(kv[bn][1], q[tn][1], Sn); SBAR(); E1; SBAR(); EX4(Sx, 4); SBAR(); \
            O[tp] = mfma(kv[bp][3], pp1, O[tp]); SBAR(); E2; SBAR(); p0_ = pack8(Sx, 0); EV; SBAR(); EX4(Sx, 8); SBAR(); O[t] = mfma(kv[b][2], p0_, O[t]); SBAR(); E3; SBAR(); \
            EX4(Sx, 12); pp1 = pack8(Sx, 1); SBAR(); } while (0)
#define WVM(n) __builtin_amdgcn_s_waitcnt(0x0F70 | ((n) & 15) | (((n) >> 4) << 14))
#define KT(b) do { \
            SUB(Sa, b, 0, Sb, b, 1, ((b) + 3) & 3, 3, gn = (gn + 1) & 15u, sk = gn * (4 * KTILE), kv[((b) + 3) & 3][0] = LDB(rWS, vK, sk), kv[((b) + 3) & 3][1] = LDB(rWS, vK + 1024, sk), ); \
            SUB(Sb, b, 1, Sa, b, 2, b, 0, sv = gn * (4 * VTILE), kv[((b) + 3) & 3][2] = LDB(rWS, vV0, sv), kv[((b) + 3) & 3][3] = LDB(rWS, vV1, sv), , ); \
            SUB(Sa, b, 2, Sb, b, 3, b, 1, , , WVM(8), , KFIX(((b) + 1) & 3)); \
            SUB(Sb, b, 3, Sa, ((b) + 1) & 3, 0, b, 2, , , , , ); } while (0)
#define KT1(b, n) do { \
            SUB(Sa, b, 0, Sb, b, 1, ((b) + 3) & 3, 3, , , , , ); \
            SUB(Sb, b, 1, Sa, b, 2, b, 0, , , , , ); \
            SUB(Sa, b, 2, Sb, b, 3, b, 1, , , , , KFIX(((b) + 1) & 3)); \
            if (rdy < 16u) { COUNT(); NEED(n); fl = __hip_atomic_load(fp, RLX_AGENT); } LD_KV(((b) + 3) & 3, (b) + 3); \
            SUB(Sb, b, 3, Sa, ((b) + 1) & 3, 0, b, 2, , , , , ); } while (0)
        KT1(0, 4u); KT1(1, 5u); KT1(2, 6u); KT1(3, 7u);
        if (rdy < 16u) { COUNT(); NEED(16u); }
        WVM(8);
        unsigned gn = (unsigned)(rb + 6) & 15u, sk, sv;
#pragma unroll 1
        for (int i = 0; i < 2; ++i) { KT(0); KT(1); KT(2); KT(3); }
        const u32x2 pre = *(volatile LAS u32x2*)(lds + C_QR);
#define KTE(b) do { SUB(Sa, b, 0, Sb, b, 1, ((b) + 3) & 3, 3, , , , , ); SUB(Sb, b, 1, Sa, b, 2, b, 0, , , , , ); SUB(Sa, b, 2, Sb, b, 3, b, 1, , , , , KFIX(((b) + 1) & 3)); SUB(Sb, b, 3, Sa, ((b) + 1) & 3, 0, b, 2, , , , , ); } while (0)
        KT(0); KTE(1); KTE(2);
        if (__builtin_expect(__builtin_amdgcn_readfirstlane(min(pre[0], pre[1])) < 4u, 0)) { cnt_wait(lds + C_QR, 4); cnt_wait(lds + C_CLR, 4); }
        asm volatile("" ::: "memory");
        float f[4];
        LAS char* md[3];
#pragma unroll
        for (int t = 0; t < 3; ++t) md[t] = lds + MG + (((w4 + 1 + t) & 3) * 3 + t) * MGT + lane * 16;
#define MGW(t, i) (*(LAS f32x4*)(md[t] + (i) * 1024) = (f32x4){O[t][4 * (i)], O[t][4 * (i) + 1], O[t][4 * (i) + 2], O[t][4 * (i) + 3]})
#define MGL(t) do { if (hh) *(LAS float*)(md[t] + 3584 - 512 - r * 12) = O[t][12]; else MGW(t, 3); } while (0)
        SUB(Sa, 3, 0, Sb, 3, 1, 2, 3, , , , , );
        SUB(Sb, 3, 1, Sa, 3, 2, 3, 0, , , , , );
        SUB(Sa, 3, 2, Sb, 3, 3, 3, 1, MGW(0, 0), MGW(0, 1), MGW(0, 2), MGL(0), );
        SUB(Sb, 3, 3, Sa, 0, 0, 3, 2, MGW(1, 0), MGW(1, 1), MGW(1, 2), MGL(1), );
        O[3] = mfma(kv[3][3], pp1, O[3]);

        PRI(3);
#pragma unroll
        for (int t = 0; t < 4; ++t) f[t] = *(const LAS float*)(lds + MREF + w4 * 512 + t * 128 + r * 4);
        MGW(2, 0); MGW(2, 1); MGW(2, 2); MGL(2);
        cnt_add(lds + C_MG, lane);
        {   const float mm = fmaxf(fmaxf(f[0], f[1]), fmaxf(f[2], f[3]));
#pragma unroll
            for (int t = 0; t < 4; ++t) f[t] = ex2(f[t] - mm); }
        const float lo = from_hi(O[3][12]) * f[3];
        asm volatile("" :: "v"(f[0]), "v"(f[1]), "v"(f[2]), "v"(lo));
        cnt_wait(lds + C_MG, 4);
        f32x16 Oo;
        bool bad;
        {   float pl[3]; f32x4 pt[3][4];
            const LAS char* s = lds + MG + w4 * 3 * MGT;
#pragma unroll
            for (int t = 0; t < 3; ++t) pl[t] = *(const LAS float*)(s + t * MGT + 3584 + r * 4);
#pragma unroll
            for (int t = 0; t < 3; ++t) {
#pragma unroll
                for (int i = 0; i < 3; ++i) pt[t][i] = *(const LAS f32x4*)(s + t * MGT + i * 1024 + lane * 16);
                pt[t][3] = *(const LAS f32x4*)(s + t * MGT + 3072 + r * 16); }
            SBAR();
            const float l = lo + pl[0] * f[0] + pl[1] * f[1] + pl[2] * f[2], sc = gq * __builtin_amdgcn_rcpf(l);
#pragma unroll
            for (int t = 0; t < 4; ++t) f[t] *= sc;
#pragma unroll
            for (int i = 0; i < 16; ++i) Oo[i] = O[3][i] * f[3] + pt[0][i >> 2][i & 3] * f[0] + pt[1][i >> 2][i & 3] * f[1] + pt[2][i >> 2][i & 3] * f[2];
            const float z = (((Oo[0] + Oo[1]) + (Oo[2] + Oo[3])) + ((Oo[4] + Oo[5]) + (Oo[6] + Oo[7]))) + (((Oo[8] + Oo[9]) + (Oo[10] + Oo[11])) + ((Oo[12] + Oo[13]) + (Oo[14] + Oo[15])));
            bad = !(l <= 1.0e30f && l >= 1.0e-30f) || !(__builtin_fabsf(z) <= 3.0e38f);
        }
        if (__builtin_expect(__any(bad), 0)) {
            u32x4 qa = q[3][0], qb = q[3][1];
            if (mO) qa[0] = 0u;
            if (mE) qb[2] = 0u;
            Oo = (f32x16){}; float m = -__builtin_inff();
#pragma unroll 1
            for (int tt = 0; tt < 64; ++tt) {
                const unsigned ko = WS_KA + o0 * 512 + lane * 16;
                const u32x4 k0 = LDB(rWS, ko, tt * KTILE), k1 = LDB(rWS, ko + 1024, tt * KTILE), v0 = LDB(rWS, WS_VA + vVl0, tt * VTILE), v1 = LDB(rWS, WS_VA + vVl1, tt * VTILE);
                f32x16 S = mfma(k0, qa, (f32x16){}); S = mfma(k1, qb, S);
                const float mn = fmaxf(m, swap_max(max16(S))), al = ex2(m - mn);
                m = mn;
#pragma unroll
                for (int i = 0; i < 16; ++i) { S[i] = ex2(S[i] - mn); Oo[i] *= al; }
                Oo = mfma(v0, pack8(S, 0), Oo); Oo = mfma(v1, pack8(S, 1), Oo);
            }
            const float sc = gq / from_hi(Oo[12]);
#pragma unroll
            for (int i = 0; i < 16; ++i) Oo[i] *= sc;
        }
        u32x4 f0 = pack8(Oo, 0), f1 = pack8(Oo, 1);
        if (hh) { f1[2] = 0u; f1[3] = 0u; }
#pragma unroll
        for (int i = 0; i < 4; ++i) { f0[i] = min(f0[i], 0xFFFFFFFEu); f1[i] = min(f1[i], 0xFFFFFFFEu); }
        const unsigned vOA = WS_OA + (tile * 16 + h) * 2048 + lane * 16;
        STB(f0, rWS, vOA, 0); STB(f1, rWS, vOA + 1024, 0);
        cnt_add(lds + C_PUB, lane);

    } else {
        PRI(2);
        LD_CH2(2);
        READ_TAG();
        u32x4 xr[28];
#define GETX(c) do { _Pragma("unroll") for (int s = 0; s < 4; ++s) xr[4 * (c) + s] = XFR(c, s); } while (0)
        wait_vm<9>(); bar(); LD_CH2(3); GETX(0);
        wait_vm<12>(); bar(); LD_CH2(4); GETX(1);
        wait_vm<12>(); bar(); LD_CH2(5); GETX(2);
        wait_vm<12>(); bar(); LD_CH2(6); GETX(3);
        wait_vm<12>(); bar();
        if (lane < 56) dma16(mksrd(gw, XROW), lane * 16, 0, L0 + GATE);
        { const unsigned vWq = (16 * w4 + (lane >> 2)) * WROW + (lane & 3) * 16 + o0 * 16;
#pragma unroll
          for (int c = 0; c < 7; ++c) dma16(sWA, vWq, KC(c) * 64 * WROW, L0 + WQ + c * 4096 + w4 * 1024); }
        GETX(4);
        wait_vm<14>(); bar(); GETX(5);
        wait_vm<8>(); bar(); LD_W2(h, W2); GETX(6);
        wait_vm<14>();
#pragma unroll
        for (int i = 0; i < 2; ++i) { const int c = 4 * i + (lane >> 4);
            if (c < 7) *(LAS unsigned short*)(lds + WQ + c * 4096 + (16 * w4 + (lane & 15)) * 64 + (odd ? 0 : 56)) = *(const LAS unsigned short*)(lds + GATE + (KC(c) * 64 + 16 * w4 + (lane & 15)) * 2); }
        unsigned hs = 0;
#define HSYNC() do { cnt_add(lds + C_H, lane); hs += 4; cnt_wait<2>(lds + C_H, hs); } while (0)
        HSYNC();

        float gq;
        {   f32x16 aQ = {};
            const LAS char* wb = lds + WQ + (8 * hh + q4) * 64 + trl; OPQ(wb);
            GEMM1(aQ, wb, true);
            gq = 1.f / (1.f + __expf(-(odd ? from_lo(aQ[0]) : from_hi(aQ[12]))));
#pragma unroll
            for (int i = 0; i < 16; ++i) aQ[i] *= QSCALE;
            u32x4 q0 = pack8n(aQ, 0), q1 = pack8n(aQ, 1);
            if (mO) { q0[0] = 0u; q0[1] = 0u; }
            if (mE) { q1[2] = 0u; q1[3] = 0u; }
            ldsw(lds + QX + w4 * 2048 + lane * 16, q0); ldsw(lds + QX + w4 * 2048 + 1024 + lane * 16, q1);
            if (!hh) *(LAS float*)(lds + GBUF + (32 * w4 + r) * 4) = gq;
            cnt_add(lds + C_Q, lane);
        }

        const bool proj = (h & 7) < 7;
        const unsigned vO = (8 * w4 + (lane >> 3)) * XROW + (h & 7) * 128 + vsw;
#define LD_WO(p, dst) do { const u32x4 sO_ = mksrd(p, DM * XROW); _Pragma("unroll") for (int i = 0; i < 14; ++i) { wait_vm<WIN - 1>(); dma16l(sO_, vO, i * 32 * XROW, L0 + (dst) + i * 4096 + w4 * 1024); } } while (0)
        u32x4 pb[4];
#define ST_V(g7) do { const unsigned tb = WS_VB + tile * VTILE, vo = tb + hh * 7168 + (64 * (g7) + r) * 16; \
            STB(pb[0], rWS, vo, 0); STB(pb[1], rWS, vo + 14336, 0); STB(pb[2], rWS, vo + 512, 0); STB(pb[3], rWS, vo + 14848, 0); \
            const unsigned c = lane == 0 ? 0x3F803F80u : 0u; STB(((u32x4){c, c, c, c}), rWS, lane < 8 ? tb + 28672 + (g7) * 128 + lane * 16 : WS_END, 0); } while (0)
        {   f32x16 a0 = {}, a1 = {};
            if (ux >= 0) { cnt_wait(lds + C_ST, 4); const unsigned vU = (16 * w4 + (lane >> 2)) * WROW + (lane & 3) * 16 + 1792 + ux * 64;
#pragma unroll
                for (int c = 0; c < 7; ++c) dma16(sWB, vU, KC(c) * 64 * WROW, L0 + c * 4096 + w4 * 1024); }
            wait_vm<0>(); HSYNC();
            if (h < 14) {
                GEMM2(W2, true);
                if (h < 7) {
#pragma unroll
                    for (int i = 0; i < 16; ++i) { a0[i] *= QSCALE; a1[i] *= QSCALE; } }
                pb[0] = pack8n(a0, 0); pb[1] = pack8n(a0, 1); pb[2] = pack8n(a1, 0); pb[3] = pack8n(a1, 1);
            } else {
                GEMM2(W2, false);
                pb[0] = pack8(a0, 0); pb[1] = pack8(a0, 1); pb[2] = pack8(a1, 0); pb[3] = pack8(a1, 1);
            }
            if (ux >= 0) {
                a0 = (f32x16){};
                const LAS char* wb = lds + (8 * hh + q4) * 64 + trl;
                GEMM1(a0, wb, false);
            }

            cnt_wait<2>(lds + C_FL, 4);
            if (h < 14) { const unsigned vo = (h < 7 ? WS_QB + h * 4096 : WS_KB + (h - 7) * 4096) + tile * KTILE + lane * 16;
                STB(pb[0], rWS, vo, 0); STB(pb[1], rWS, vo + 1024, 0); STB(pb[2], rWS, vo + 2048, 0); STB(pb[3], rWS, vo + 3072, 0);
            } else ST_V(h - 14);
            if (ux >= 0) { const unsigned tb = WS_VB + tile * VTILE, vo = tb + hh * 7168 + (32 * ux + r) * 16;
                STB(pack8(a0, 0), rWS, vo, 0); STB(pack8(a0, 1), rWS, vo + 14336, 0);
                const unsigned c = lane == 0 ? 0x3F803F80u : 0u; STB(((u32x4){c, c, c, c}), rWS, lane < 8 ? tb + 28672 + (ux >> 1) * 128 + lane * 16 : WS_END, 0); }
        }
        cnt_add(lds + C_H, lane); hs += 4;
        { const u32x4 ones = {~0u, ~0u, ~0u, ~0u}; const unsigned vo = WS_OA + (tile * 16 + h) * 2048 + lane * 16;
          STB(ones, rWS, vo, 0); STB(ones, rWS, vo + 1024, 0); }
        wait_vm<0>();
        if (lane == 0 || (lane == 1 && h >= 14) || (lane == 2 && ux >= 0)) __hip_atomic_store(FGP(tile, lane == 2 ? 28 + ux : h < 14 ? 14 + h : 2 * h + lane), tag, RLX_AGENT);
        cnt_add(lds + C_CLR, lane);

        cnt_wait<2>(lds + C_H, hs);
        if (proj) LD_WO(woB, WOB);
        const int rt = w4 & 1, kh = w4 >> 1, T = rb * 4 + (h >> 3) * 2 + rt;
        const LAS char* wo0 = lds + kh * 28672 + (4 * hh + q4) * 128 + trl + (q4 >> 1) * 64;
        const LAS char* wo1 = lds + kh * 28672 + (4 * hh + q4) * 128 + trl + ((q4 >> 1) ^ 1) * 64;
        const unsigned wfix = 3072 - hh * 1024;
#define WOF(b, bx, hd, s) ((s) == 0 ? tr2((b) + (hd) * 3584, (b) + (hd) * 3584 + 1024) : tr2((b) + (hd) * 3584 + 2048, (bx) + (hd) * 3584))
        {   const int i = lane & 7, g = i == 0 ? 14 + ga : i == 1 ? 14 + gb : i == 2 ? 21 + ga : i == 3 ? 21 + gb : i == 4 ? 28 + ((28 * h) >> 5) : 28 + ((28 * h + 27) >> 5);
            const int tp = tile > 0 ? tile - 1 : 0;
            const int tf = (lane & 8) ? tp : tile;
            poll_flags<24>(FGP(tf, g), tag);

            const unsigned vk = o0 * 512 + lane * 16;
            u32x4 b0 = LDB(rWS, vk, WS_QB + tile * KTILE), b1 = LDB(rWS, vk + 1024, WS_QB + tile * KTILE);
            const u32x4 kp0 = LDB(rWS, vk, WS_KB + tp * KTILE), kp1 = LDB(rWS, vk + 1024, WS_KB + tp * KTILE), ko0 = LDB(rWS, vk, WS_KB + tile * KTILE), ko1 = LDB(rWS, vk + 1024, WS_KB + tile * KTILE);
            const u32x4 vp1 = LDB(rWS, vVl1, WS_VB + tp * VTILE), vo0 = LDB(rWS, vVl0, WS_VB + tile * VTILE), vo1 = LDB(rWS, vVl1, WS_VB + tile * VTILE);
            if (mO) { b0[0] = 0u; b0[1] = 0u; }
            if (mE) { b1[2] = 0u; b1[3] = 0u; }
            f32x16 A = mfma(kp0, b0, (f32x16){}); A = mfma(kp1, b1, A);
            f32x16 B = mfma(ko0, b0, (f32x16){}); B = mfma(ko1, b1, B);
            const u64 M = 0x5555ull << (18 + r);
            unsigned Mp = (unsigned)M >> (4 * hh); const unsigned Mo = (unsigned)(M >> 32) >> (4 * hh);
            if (tile == 0) Mp = 0u;
#pragma unroll
            for (int i2 = 0; i2 < 16; ++i2) { const int c = (i2 & 3) + 8 * (i2 >> 2);
                const unsigned tb = (unsigned)__builtin_amdgcn_sbfe((int)Mo, c, 1); B[i2] = __uint_as_float((__float_as_uint(B[i2]) & tb) | (0xff800000u & ~tb));
                if (i2 >= 8) { const unsigned ta = (unsigned)__builtin_amdgcn_sbfe((int)Mp, c, 1); A[i2] = __uint_as_float((__float_as_uint(A[i2]) & ta) | (0xff800000u & ~ta)); } }
            float m = max16(B);
            m = MX3(m, A[8], A[9]); m = MX3(m, A[10], A[11]); m = MX3(m, A[12], A[13]); m = MX3(m, A[14], A[15]);
            m = swap_max(m);
#pragma unroll
            for (int i2 = 0; i2 < 16; ++i2) { B[i2] = ex2(B[i2] - m); if (i2 >= 8) A[i2] = ex2(A[i2] - m); }
            f32x16 OB = mfma(vp1, pack8(A, 1), (f32x16){});
            OB = mfma(vo0, pack8(B, 0), OB); OB = mfma(vo1, pack8(B, 1), OB);
            const float sc = (1.f - gq) * __builtin_amdgcn_rcpf(from_hi(OB[12]));
#pragma unroll
            for (int i2 = 0; i2 < 16; ++i2) OB[i2] *= sc;
            u32x4 f0 = pack8(OB, 0), f1 = pack8(OB, 1);
            if (hh) { f1[2] = 0u; f1[3] = 0u; }
            const unsigned vo = WS_OB + (tile * 16 + h) * 2048 + lane * 16;
            STB(f0, rWS, vo, 0); STB(f1, rWS, vo + 1024, 0);
        }
        wait_vm<0>();
        if (lane == 0) __hip_atomic_store((GAS u64*)(ws + WS_FOB + (tile * 16 + h) * 8), tag, RLX_AGENT);

        if (proj) {
            cnt_add(lds + C_H, lane); hs += 4;
            LD_WO(woA, WOA);
            poll_flags<24>((const GAS u64*)(ws + WS_FOB + (T * 16 + 8 * kh + (lane & 7)) * 8), tag);
            cnt_wait<2>(lds + C_H, hs);
            f32x16 c0 = {}, c1 = {};
            const unsigned vF = T * 32768 + kh * 16384 + lane * 16;
            {   const LAS char* p0x = wo0 + wfix; const LAS char* p1x = wo1 + wfix;
                u32x4 of[16];
                SBAR(); PRI(0);
#pragma unroll
                for (int i = 0; i < 16; ++i) of[i] = LDB(rWS, vF, WS_OB + i * 1024);
                SBAR(); PRI(2);
#pragma unroll
                for (int i = 0; i < 16; ++i) { c0 = mfma(WOF(wo0, p0x, i >> 1, i & 1), of[i], c0); c1 = mfma(WOF(wo1, p1x, i >> 1, i & 1), of[i], c1); SBAR(); }
            }
            wait_vm<0>(); HSYNC();
            u32x4 wf[8][2][2];
            {   const LAS char* p0 = wo0 + WOA; const LAS char* p1 = wo1 + WOA; const LAS char* p0x = p0 + wfix; const LAS char* p1x = p1 + wfix; OPQ(p0); OPQ(p1); OPQ(p0x); OPQ(p1x);
#pragma unroll
                for (int hd = 0; hd < 8; ++hd) { wf[hd][0][0] = WOF(p0, p0x, hd, 0); wf[hd][1][0] = WOF(p0, p0x, hd, 1); wf[hd][0][1] = WOF(p1, p1x, hd, 0); wf[hd][1][1] = WOF(p1, p1x, hd, 1); }
            }

            cnt_wait<4>(lds + C_PUB, 4);
            __builtin_amdgcn_s_sleep(8);

            unsigned nx = 0;
#pragma unroll 1
            for (unsigned sp = 0; sp < kSpin && nx < 8; ++sp) {
                asm volatile("" ::: "memory");
                u32x4 of[16];
#pragma unroll
                for (int i = 0; i < 16; ++i) of[i] = LDB(rWS, vF, (unsigned)(i >> 1) >= nx ? WS_OA + i * 1024 : WS_END);
#pragma unroll
                for (int hd = 0; hd < 8; ++hd) {
                    const u32x4 a = of[2 * hd], b = of[2 * hd + 1];
                    const unsigned mx = max(max(max(a[0], a[1]), max(a[2], a[3])), max(max(b[0], b[1]), max(b[2], b[3])));
                    const unsigned ok = (unsigned)__all(mx != ~0u) | (unsigned)(sp + 1 == kSpin);
                    if ((unsigned)(nx == (unsigned)hd) & ok) {
                        c0 = mfma(wf[hd][0][0], a, c0); c1 = mfma(wf[hd][0][1], a, c1); c0 = mfma(wf[hd][1][0], b, c0); c1 = mfma(wf[hd][1][1], b, c1);
                        nx = hd + 1;
                    }
                }
            }
            const f32x16 give = kh ? c0 : c1, keep = kh ? c1 : c0;
            { LAS char* d = lds + WOB + w4 * 4096 + lane * 16;
#pragma unroll
              for (int i = 0; i < 4; ++i) *(LAS f32x4*)(d + i * 1024) = (f32x4){give[4 * i], give[4 * i + 1], give[4 * i + 2], give[4 * i + 3]}; }
            cnt_add(lds + C_PAIR + rt * 4, lane); cnt_wait(lds + C_PAIR + rt * 4, 2);
            f32x16 fin;
            { const LAS char* s = lds + WOB + (w4 ^ 2) * 4096 + lane * 16;
#pragma unroll
              for (int i = 0; i < 4; ++i) { const f32x4 t = *(const LAS f32x4*)(s + i * 1024);
#pragma unroll
                  for (int k = 0; k < 4; ++k) fin[4 * i + k] = keep[4 * i + k] + t[k]; } }
            GAS char* orow = (GAS char*)out + (T * 32 + r) * XROW + (h & 7) * 128 + kh * 64 + hh * 16;
            *(GAS u32x4*)orow = pack8n(fin, 0); *(GAS u32x4*)(orow + 32) = pack8n(fin, 1);
        } else if (h == 7 && rb == 0 && wave == 4) {
#pragma unroll 1
            for (int i = 0; i < 4; ++i) { const int hq = i * 4 + (lane >> 4); poll_flags<24>(FGP((lane & 15) * 4, hq < 15 ? 14 + hq : 30), tag); }
            if (lane == 0) __hip_atomic_store((GAS u64*)(ws + WS_EPOCH), tag, RLX_AGENT);
        }
    }
}

extern "C" void kernel_launch(void* const* d_in, const int* in_sizes, int n_in, void* d_out, int out_size, void* d_ws, size_t ws_size, hipStream_t stream) {
    fused_kernel<<<256, 512, 0, stream>>>((const char*)d_in[0], (const char*)d_in[1], (const char*)d_in[2], (const char*)d_in[4], (const char*)d_in[5],
                                          (const char*)d_in[6], (char*)d_out, (char*)d_ws);
}
```
